```python
import torch

def _softmax(x, axis=-1):
    return torch.softmax(x, dim=axis)
def _gelu(x):
    return torch.nn.functional.gelu(x, approximate="tanh")
def _silu(x):
    return torch.nn.functional.silu(x)
def _sigmoid(x):
    return torch.sigmoid(x)
def _layernorm(x, axis=-1, eps=1e-5):
    m = x.mean(dim=axis, keepdim=True)
    v = x.var(dim=axis, keepdim=True, unbiased=False)
    return (x - m) / torch.sqrt(v + eps)
def _scatter_add(tgt, idx, src, axis=0):
    return tgt.clone().index_add_(axis, idx, src)
def _segment_sum(src, seg, n):
    out = torch.zeros((n,) + src.shape[1:], dtype=src.dtype)
    return out.index_add_(0, seg, src)
def _pad(x, pw):
    flat = tuple(v for pair in reversed(pw) for v in pair)
    return torch.nn.functional.pad(x, flat)

kernel_name = 'sequential_preLN__mha_block__geglu_ffn__prompt'

def setup_inputs():

    _w = (lambda a: (lambda t: t.to(torch.bfloat16) if t.is_floating_point() else t)(torch.as_tensor(a)))
    def _A_setup():
        B, S, D, H = 1, 2048, 576, 32
        return {
            "x": _w((lambda *s: torch.randn(*s, dtype=torch.bfloat16) * 0.05)(B, S, D)),
            "wqkv": _w((lambda *s: torch.randn(*s, dtype=torch.bfloat16) * 0.05)(D, 3 * D)),
            "wo": _w((lambda *s: torch.randn(*s, dtype=torch.bfloat16) * 0.05)(D, D)),
        }
    _A_in = _A_setup()

    def _B_setup():
        B, S, D, F = 1, 2048, 576, 4608
        return {
            "x": _w((lambda *s: torch.randn(*s, dtype=torch.bfloat16) * 0.05)(B, S, D)),
            "w_gate": _w((lambda *s: torch.randn(*s, dtype=torch.bfloat16) * 0.05)(D, F)),
            "w_up": _w((lambda *s: torch.randn(*s, dtype=torch.bfloat16) * 0.05)(D, F)),
            "w_down": _w((lambda *s: torch.randn(*s, dtype=torch.bfloat16) * 0.05)(F, D)),
        }
    _B_in = _B_setup()

    return {
        "A_x": _A_in["x"],
        "A_wqkv": _A_in["wqkv"],
        "A_wo": _A_in["wo"],
        "B_x": _B_in["x"],
        "B_w_gate": _B_in["w_gate"],
        "B_w_up": _B_in["w_up"],
        "B_w_down": _B_in["w_down"],
    }

def reference(A_x, A_wqkv, A_wo, B_x, B_w_gate, B_w_up, B_w_down):
    def _A_ref(x, wqkv, wo):
        B, S, D = x.shape
        H = 32
        Dh = D // H
        qkv = (lambda x, *d: x.permute(*d))((x @ wqkv).reshape(B, S, 3, H, Dh), 2, 0, 3, 1, 4)
        q, k, v = qkv[0], qkv[1], qkv[2]
        scores = torch.einsum("bhqd,bhkd->bhqk", q, k) * (Dh ** -0.5)
        p = _softmax(scores, axis=-1)
        o = torch.einsum("bhqk,bhkd->bhqd", p, v)
        return (lambda x, *d: x.permute(*d))(o, 0, 2, 1, 3).reshape(B, S, D) @ wo

    def _B_ref(x, w_gate, w_up, w_down):
        return (_gelu(x @ w_gate) * (x @ w_up)) @ w_down

    x = A_x
    h = x + _A_ref(_layernorm(x, axis=-1), A_wqkv, A_wo)
    h2 = h + _B_ref(_layernorm(h, axis=-1), B_w_gate, B_w_up, B_w_down)
    return h2
```

```cpp
#include <hip/hip_runtime.h>
#include <hip/hip_bf16.h>
#include <stdint.h>

typedef unsigned short u16;
typedef short bf16x8 __attribute__((ext_vector_type(8)));
typedef short bf16x4 __attribute__((ext_vector_type(4)));
typedef float f32x2 __attribute__((ext_vector_type(2)));
typedef float f32x4 __attribute__((ext_vector_type(4)));
typedef float f32x16 __attribute__((ext_vector_type(16)));
typedef unsigned u32x2 __attribute__((ext_vector_type(2)));
typedef unsigned u32x4 __attribute__((ext_vector_type(4)));
typedef __bf16 bf16x2n __attribute__((ext_vector_type(2)));
#define LAS __attribute__((address_space(3)))
#define GAS __attribute__((address_space(1)))
#define DI __device__ __forceinline__
#define SBAR() __builtin_amdgcn_sched_barrier(0)
#define PIN(x) asm volatile("" : "+v"(x))

constexpr int S_ = 2048, D_ = 576, F_ = 4608;
constexpr float LN_EPS = 1e-5f;
constexpr float QSCALE = 0.23570226039551584f * 1.4426950408889634f;

constexpr size_t WS_QM = 0;
constexpr size_t WS_Q2 = WS_QM + (size_t)32 * 2048 * 32;
constexpr size_t WS_KV = WS_Q2 + (size_t)16 * 2048 * 8;
constexpr size_t WS_OF = WS_KV + (size_t)32 * 64 * 2560;
constexpr size_t WS_HF = WS_OF + (size_t)S_ * D_ * 2;
constexpr size_t WS_ST = WS_HF + (size_t)S_ * D_ * 2;
constexpr size_t WS_CS = WS_ST + (size_t)S_ * 64;
constexpr size_t WS_WO = WS_CS + (size_t)576 * 18 * 64;
constexpr size_t WS_WD = WS_WO + (size_t)8 * 9 * 9216;
constexpr size_t WS_GU = WS_WD + (size_t)8 * 72 * 9216;
constexpr size_t WS_AF = WS_GU + (size_t)192 * 54 * 1024;

DI unsigned pk2(float a, float b) { f32x2 v = {a, b}; return __builtin_bit_cast(unsigned, __builtin_convertvector(v, bf16x2n)); }
DI float bflo(unsigned u) { return __uint_as_float(u << 16); }
DI float bfhi(unsigned u) { return __uint_as_float(u & 0xffff0000u); }
DI f32x4 mfma16(u32x4 a, u32x4 b, f32x4 c) { return __builtin_amdgcn_mfma_f32_16x16x32_bf16(__builtin_bit_cast(bf16x8, a), __builtin_bit_cast(bf16x8, b), c, 0, 0, 0); }
DI f32x16 mfma32(u32x4 a, u32x4 b, f32x16 c) { return __builtin_amdgcn_mfma_f32_32x32x16_bf16(__builtin_bit_cast(bf16x8, a), __builtin_bit_cast(bf16x8, b), c, 0, 0, 0); }
DI f32x16 mfma32k8(u32x2 a, u32x2 b, f32x16 c) { return __builtin_amdgcn_mfma_f32_32x32x8bf16_1k(__builtin_bit_cast(bf16x4, a), __builtin_bit_cast(bf16x4, b), c, 0, 0, 0); }
DI float dot2(unsigned a, unsigned b, float c) { return __builtin_amdgcn_fdot2_f32_bf16(__builtin_bit_cast(bf16x2n, a), __builtin_bit_cast(bf16x2n, b), c, false); }
DI void glds16(const void* g, unsigned lds_base) {
    unsigned sv;
    asm volatile("s_mov_b32 %0, m0\n\ts_mov_b32 m0, %2\n\ts_nop 0\n\tglobal_load_lds_dwordx4 %1, off\n\ts_mov_b32 m0, %0" : "=&s"(sv) : "v"(g), "s"(lds_base) : "memory");
}
#define WAIT_BAR(N) asm volatile("s_waitcnt vmcnt(" #N ") lgkmcnt(0)\n\ts_barrier" ::: "memory")
DI int swap23(int v) { return (v & ~12) | ((v & 4) << 1) | ((v & 8) >> 1); }
DI int vpos(int r) { return r < 4 ? r : r < 11 ? r + 4 : r == 11 ? 19 : r < 16 ? r - 8 : r < 19 ? r : 15; }
DI float xor16(float v) { return __shfl_xor(v, 16, 64); }
DI float xor32(float v) { return __shfl_xor(v, 32, 64); }

constexpr int K1_LDS = 138240 + 512 + 1024;
__global__ __launch_bounds__(256, 1) void k1_qkv(const u16* __restrict__ x, const u16* __restrict__ wqkv, char* __restrict__ ws) {
    __shared__ __attribute__((aligned(16))) char lds[K1_LDS];
    const int b = blockIdx.x, hp = 2 * (b & 7) + ((b >> 3) & 1), mtile = b >> 4;
    const int tid = threadIdx.x, l = tid & 63, w = __builtin_amdgcn_readfirstlane(tid >> 6);
    const int i16 = l & 15, q = l >> 4, odd = hp & 1;
    const int row0 = mtile * 128 + w * 32;
    const unsigned lds0 = (unsigned)(uintptr_t)lds;
    u32x4 xf[2][18];
    {
        const GAS char* xp = (const GAS char*)x + ((size_t)(row0 + i16) * D_ + q * 8) * 2;
#pragma unroll
        for (int ks = 0; ks < 18; ++ks)
#pragma unroll
            for (int m = 0; m < 2; ++m) xf[m][ks] = *(const GAS u32x4*)(xp + m * 16 * D_ * 2 + ks * 64);
    }
    SBAR();
    {
        const char* src[4]; int uu[4];
#pragma unroll
        for (int j = 0; j < 4; ++j) {
            const int u = (w + 4 * j) < 15 ? (w + 4 * j) : 14; uu[j] = u;
            const int idx = 64 * u + l, blk = idx / 60, rem = idx - blk * 60, c = rem >> 2, a = rem & 3, seg = c / 5, cc = c - seg * 5;
            src[j] = (const char*)wqkv + ((size_t)(4 * blk + a) * 1728 + seg * 576 + 36 * hp - 4 * odd + 8 * cc) * 2;
        }
#pragma unroll
        for (int t = 0; t < 9; ++t)
#pragma unroll
            for (int j = 0; j < 4; ++j) glds16(src[j] + (size_t)t * 64 * 1728 * 2, lds0 + t * 15360 + uu[j] * 1024);
    }
    const int p = l & 3, a4 = i16 >> 2;
    auto tbase = [&](int T) {
        int seg, g;
        if (T == 4) { seg = p == 0 ? 0 : 1; g = 4; }
        else if (T == 7) { seg = 2; g = 4; }
        else { const int tt = T > 4 ? T - 1 : T; seg = tt >> 1; g = (tt & 1) * 5 + p; }
        const int o8 = g + odd;
        return (const LAS char*)lds + q * 1920 + (seg * 5 + (o8 >> 1)) * 64 + a4 * 16 + (o8 & 1) * 8;
    };
    typedef short v4s __attribute__((ext_vector_type(4)));
    auto wfrag = [&](const LAS char* base, int ks) {
        const v4s lo = __builtin_amdgcn_ds_read_tr16_b64_v4i16((LAS v4s*)(base + ks * 7680));
        const v4s hi = __builtin_amdgcn_ds_read_tr16_b64_v4i16((LAS v4s*)(base + ks * 7680 + 960));
        const u32x2 a = __builtin_bit_cast(u32x2, lo), c = __builtin_bit_cast(u32x2, hi);
        return u32x4{a[0], a[1], c[0], c[1]};
    };
    const LAS char* tb[8];
#pragma unroll
    for (int T = 0; T < 8; ++T) tb[T] = tbase(T);
    const LAS char* cb0 = tbase(2 * w), * cb1 = tbase(2 * w + 1);
    f32x4 acc[8][2], c0 = {0, 0, 0, 0}, c1 = {0, 0, 0, 0};
#pragma unroll
    for (int T = 0; T < 8; ++T) acc[T][0] = acc[T][1] = f32x4{0, 0, 0, 0};
    float s1[2] = {0.f, 0.f}, s2[2] = {0.f, 0.f};
    const u32x4 ones = {0x3f803f80u, 0x3f803f80u, 0x3f803f80u, 0x3f803f80u};
#pragma unroll
    for (int ks = 0; ks < 18; ++ks) {
        if (ks == 0) WAIT_BAR(24);
        if (ks == 6) WAIT_BAR(12);
        if (ks == 12) WAIT_BAR(0);
#pragma unroll
        for (int T = 0; T < 8; ++T) {
            const u32x4 wf = wfrag(tb[T], ks);
#pragma unroll
            for (int m = 0; m < 2; ++m) acc[T][m] = T < 5 ? mfma16(wf, xf[m][ks], acc[T][m]) : mfma16(xf[m][ks], wf, acc[T][m]);
        }
        c0 = mfma16(wfrag(cb0, ks), ones, c0);
        c1 = mfma16(wfrag(cb1, ks), ones, c1);
#pragma unroll
        for (int m = 0; m < 2; ++m)
#pragma unroll
            for (int e = 0; e < 4; ++e) { s1[m] = dot2(xf[m][ks][e], 0x3f803f80u, s1[m]); s2[m] = dot2(xf[m][ks][e], xf[m][ks][e], s2[m]); }
    }
    LAS float* cs = (LAS float*)(lds + 138240);
    LAS f32x2* st = (LAS f32x2*)(lds + 138240 + 512) + w * 32;
    if (i16 == 0) { *(LAS f32x4*)(cs + (2 * w) * 16 + 4 * q) = c0; *(LAS f32x4*)(cs + (2 * w + 1) * 16 + 4 * q) = c1; }
    float rs[2], nm[2];
#pragma unroll
    for (int m = 0; m < 2; ++m) {
        float a = s1[m], c = s2[m];
        a += xor16(a); a += xor32(a); c += xor16(c); c += xor32(c);
        const float mean = a * (1.f / D_), var = fmaxf(c * (1.f / D_) - mean * mean, 0.f);
        rs[m] = rsqrtf(var + LN_EPS); nm[m] = -mean;
        if (q == 0) st[m * 16 + i16] = f32x2{rs[m], nm[m]};
    }
    __syncthreads();
    const int tile = row0 >> 5;
    char* kv0 = ws + WS_KV + ((size_t)(2 * hp) * 64 + tile) * 2560;
#pragma unroll
    for (int m = 0; m < 2; ++m) {
        const int tok = row0 + m * 16 + i16, rho = swap23(m * 16 + i16);
#pragma unroll
        for (int T = 0; T < 5; ++T) {
            const f32x4 c = *(const LAS f32x4*)(cs + T * 16 + 4 * q);
            const float sc = (T < 2 || (T == 4 && q == 0)) ? rs[m] * QSCALE : rs[m];
            f32x4 v;
#pragma unroll
            for (int r = 0; r < 4; ++r) v[r] = (acc[T][m][r] + nm[m] * c[r]) * sc;
            const u32x2 o = {pk2(v[0], v[1]), pk2(v[2], v[3])};
            char* dst;
            if (T < 2) dst = ws + WS_QM + ((size_t)(2 * hp + T) * 2048 + tok) * 32 + q * 8;
            else if (T < 4) dst = kv0 + (size_t)(T - 2) * 64 * 2560 + (q >> 1) * 512 + rho * 16 + (q & 1) * 8;
            else dst = q == 0 ? ws + WS_Q2 + ((size_t)hp * 2048 + tok) * 8 : kv0 + (size_t)(q - 1) * 64 * 2560 + 1024 + rho * 8;
            if (T < 4 || q < 3) *(GAS u32x2*)dst = o;
        }
        f32x2 sv[4];
#pragma unroll
        for (int r = 0; r < 4; ++r) sv[r] = st[m * 16 + 4 * q + r];
#pragma unroll
        for (int T = 5; T < 8; ++T) {
            const float c = cs[T * 16 + i16];
            f32x4 v;
#pragma unroll
            for (int r = 0; r < 4; ++r) v[r] = (acc[T][m][r] + sv[r][1] * c) * sv[r][0];
            u32x2 o = {pk2(v[0], v[1]), pk2(v[2], v[3])};
            int hd, pos;
            if (T < 7) { hd = T - 5; pos = vpos(swap23(i16)); }
            else {
                hd = i16 < 4 ? i16 >> 1 : i16 & 1; pos = i16 < 4 ? 16 + (i16 & 1) : i16 < 6 ? 18 : 15;
                if (i16 >= 4) { const unsigned f = i16 < 6 ? 0x3f803f80u : 0u; o = u32x2{f, f}; }
            }
            char* dst = kv0 + (size_t)hd * 64 * 2560 + 1280 + (m * 2 + (q >> 1)) * 320 + pos * 16 + (q & 1) * 8;
            if (T < 7 || i16 < 8) *(GAS u32x2*)dst = o;
        }
    }
}

DI void wd_rows(int kap, int& A, int& B) {
    const int W = kap / 3, qq = kap - 3 * W, T0 = 3 * W + (qq == 2 ? 2 : 0), T1 = 3 * W + (qq == 2 ? 2 : 1);
    A = 64 * (T0 >> 3) + (T0 & 7) + (qq == 2 ? 0 : 16 * qq);
    B = 64 * (T1 >> 3) + (T1 & 7) + (qq == 2 ? 16 : 16 * qq);
}
DI void wo_rows(int kap, int& A, int& B) {
    const int hd = kap >> 1;
    A = kap < 64 ? 18 * hd + 2 * (hd & 1) + 8 * (kap & 1) : 72 * (kap - 64) + 16;
    B = A + (kap < 64 ? 4 : 36);
}

__global__ __launch_bounds__(512, 2) void k2_attn(const u16* __restrict__ wo, const u16* __restrict__ wg, const u16* __restrict__ wu,
                                                  const u16* __restrict__ wd, char* __restrict__ ws) {
    __shared__ __attribute__((aligned(16))) char lds[163840];
    const int b = blockIdx.x, xcd = b & 7, bi = b >> 3, head = 4 * xcd + (bi & 3), qb = bi >> 2;
    const int tid = threadIdx.x, l = tid & 63, w = __builtin_amdgcn_readfirstlane(tid >> 6);
    const int r32 = l & 31, h = l >> 5;
    const unsigned lds0 = (unsigned)(uintptr_t)lds;
    const int qrow = qb * 256 + w * 32 + r32;
    const u32x4 q1 = *(const GAS u32x4*)(ws + WS_QM + ((size_t)head * 2048 + qrow) * 32 + h * 16);
    u32x2 q2 = *(const GAS u32x2*)(ws + WS_Q2 + ((size_t)(head >> 1) * 2048 + qrow) * 8);
    const char* kvsrc = ws + WS_KV + (size_t)head * 163840 + w * 1024 + l * 16;
#pragma unroll
    for (int j = 0; j < 6; ++j) glds16(kvsrc + j * 8192, lds0 + w * 1024 + j * 8192);
    const int v = bi * 8 + w;
    const int kq = l >> 4, i16 = l & 15;
    const char* sp[2][2];
    long s0, s1;
    char* sdst; int dstep; int kind;
    if (v < 162) {
        kind = 0;
        const int u = 9 * xcd + v / 18, ks = v % 18;
        sp[0][0] = (const char*)(((i16 >> 1) & 1) ? wu : wg) + ((32 * ks + 8 * kq) * F_ + 64 * u + 8 * (2 * (i16 >> 2) + (i16 & 1))) * 2;
        sp[0][1] = sp[0][0] + 4 * F_ * 2; sp[1][0] = sp[0][0] + 8; sp[1][1] = sp[0][1] + 8;
        s0 = F_ * 2; s1 = 2 * F_ * 2;
        sdst = ws + WS_GU + (size_t)ks * 3072 + l * 16; dstep = 8 * u;
    } else {
        const int idx = xcd * 94 + (v - 162);
        const bool isd = idx < 648; const int id2 = isd ? idx : idx - 648, nmain = isd ? 576 : 72, npair = isd ? 72 : 9;
        kind = id2 < nmain ? 1 : (id2 < nmain + npair ? 2 : 3);
        const int pr = kind == 1 ? id2 >> 3 : (kind == 2 ? id2 - nmain : 0), nb = kind == 1 ? (id2 & 7) : (i16 & 7);
#pragma unroll
        for (int c = 0; c < 2; ++c) {
            const int par = kind == 1 ? c : (i16 >> 3), kap = 8 * pr + 4 * par + kq;
            int A, B, A2, B2;
            wd_rows(kap, A, B); wo_rows(kap, A2, B2);
            const char* bp = (const char*)(isd ? wd : wo) + (72 * nb + (kind == 1 ? 4 * i16 : 64 + 4 * c)) * 2;
            sp[c][0] = bp + (isd ? A : A2) * (D_ * 2); sp[c][1] = bp + (isd ? B : B2) * (D_ * 2);
        }
        s0 = (isd ? 8 : 1) * D_ * 2; s1 = (isd ? 32 : 2) * D_ * 2;
        sdst = ws + (isd ? WS_WD : WS_WO) + ((size_t)nb * npair + pr) * 9216 + (kind == 1 ? l * 16 : 8192 + kq * 256 + (i16 >> 3) * 128);
        dstep = kind == 1 ? 1024 : 16;
    }
    if (head & 1) q2[0] = 0; else q2[1] = 0;
    if (h) q2 = u32x2{0, 0};
    PIN(q2);
    { u32x4 t = q1; PIN(t); }
    WAIT_BAR(0);
#pragma unroll
    for (int j = 6; j < 20; ++j) glds16(kvsrc + j * 8192, lds0 + w * 1024 + j * 8192);
    u32x2 sd[16];
#pragma unroll
    for (int j = 0; j < 16; ++j)
        asm volatile("global_load_dwordx2 %0, %1, off" : "=v"(sd[j]) : "v"(sp[j & 1][j >> 3] + ((j >> 2) & 1) * s1 + ((j >> 1) & 1) * s0) : "memory");
    SBAR();

    const LAS char* const kp0 = (const LAS char*)lds + h * 512 + r32 * 16;
    const LAS char* const k2p0 = (const LAS char*)lds + 1024 + r32 * 8;
    const LAS char* const vp0 = (const LAS char*)lds + 1280 + h * 320 + vpos(r32) * 16;
    f32x16 o;
    float lsum;
    for (int attempt = 0;; ++attempt) {
        float mx = -3.0e38f;
        const int nt = attempt ? 64 : 1;
        for (int t = 0; t < nt; ++t) {
            f32x16 s = mfma32k8(*(const LAS u32x2*)(k2p0 + t * 2560), q2, f32x16{});
            s = mfma32(*(const LAS u32x4*)(kp0 + t * 2560), q1, s);
#pragma unroll
            for (int r = 0; r < 16; ++r) mx = fmaxf(mx, s[r]);
        }
        mx = fmaxf(mx, xor32(mx));
        f32x16 negm;
#pragma unroll
        for (int r = 0; r < 16; ++r) negm[r] = -mx;
        PIN(negm);
        o = f32x16{};
        const LAS char* kp = kp0; const LAS char* k2p = k2p0; const LAS char* vp = vp0;
        f32x16 sa, sb;
        sa = mfma32(*(const LAS u32x4*)kp, q1, mfma32k8(*(const LAS u32x2*)k2p, q2, negm));
        u32x4 kf = *(const LAS u32x4*)(kp + 2560); u32x2 k2 = *(const LAS u32x2*)(k2p + 2560);
        u32x4 v0 = *(const LAS u32x4*)vp, v1 = *(const LAS u32x4*)(vp + 640);
#define EX(x) __builtin_amdgcn_exp2f(x)
#define STEP(C, N, KP, K2P, VP) do { u32x4 p0, p1; \
            N = mfma32k8(k2, q2, negm); k2 = *(const LAS u32x2*)(K2P); \
            C[0] = EX(C[0]); C[1] = EX(C[1]); C[2] = EX(C[2]); C[3] = EX(C[3]); p0[0] = pk2(C[0], C[1]); p0[1] = pk2(C[2], C[3]); PIN(p0[0]); PIN(p0[1]); SBAR(); \
            N = mfma32(kf, q1, N); kf = *(const LAS u32x4*)(KP); \
            C[4] = EX(C[4]); C[5] = EX(C[5]); C[6] = EX(C[6]); C[7] = EX(C[7]); p0[2] = pk2(C[4], C[5]); p0[3] = pk2(C[6], C[7]); \
            C[8] = EX(C[8]); C[9] = EX(C[9]); PIN(p0); PIN(C[8]); PIN(C[9]); SBAR(); \
            o = mfma32(v0, p0, o); v0 = *(const LAS u32x4*)(VP); \
            C[10] = EX(C[10]); C[11] = EX(C[11]); C[12] = EX(C[12]); C[13] = EX(C[13]); C[14] = EX(C[14]); C[15] = EX(C[15]); \
            p1[0] = pk2(C[8], C[9]); p1[1] = pk2(C[10], C[11]); p1[2] = pk2(C[12], C[13]); p1[3] = pk2(C[14], C[15]); PIN(p1); SBAR(); \
            o = mfma32(v1, p1, o); v1 = *(const LAS u32x4*)((VP) + 640); SBAR(); } while (0)
        for (int it = 0; it < 8; ++it) {
            if (attempt == 0) {
                if (it == 2) WAIT_BAR(16);
                if (it == 4 && kind != 3) {
                    asm volatile("s_waitcnt vmcnt(0)" : "+v"(sd[0]), "+v"(sd[1]), "+v"(sd[2]), "+v"(sd[3]), "+v"(sd[4]), "+v"(sd[5]), "+v"(sd[6]), "+v"(sd[7]),
                                 "+v"(sd[8]), "+v"(sd[9]), "+v"(sd[10]), "+v"(sd[11]), "+v"(sd[12]), "+v"(sd[13]), "+v"(sd[14]), "+v"(sd[15]));
                    f32x4 csum[8];
#pragma unroll
                    for (int t = 0; t < 8; ++t) {
                        u32x4 f;
#pragma unroll
                        for (int a = 0; a < 4; ++a)
                            f[a] = __builtin_amdgcn_perm(sd[4 * a + 2 + (t >> 2)][(t >> 1) & 1], sd[4 * a + (t >> 2)][(t >> 1) & 1], (t & 1) ? 0x07060302u : 0x05040100u);
                        if (kind == 0) {
                            const int T = dstep + t, W = T / 3;
                            *(GAS u32x4*)(sdst + (size_t)W * 55296 + (T - 3 * W) * 1024) = f;
                            csum[t] = mfma16(f, u32x4{0x3f803f80u, 0x3f803f80u, 0x3f803f80u, 0x3f803f80u}, f32x4{0, 0, 0, 0});
                        } else *(GAS u32x4*)(sdst + t * dstep) = f;
                    }
                    if (kind == 0 && i16 == 0) {
#pragma unroll
                        for (int t = 0; t < 8; ++t) *(GAS f32x4*)(ws + WS_CS + ((size_t)(dstep + t) * 18 + v % 18) * 64 + kq * 16) = csum[t];
                    }
                }
            }
            const int dl = it == 7 ? -56 * 2560 : 8 * 2560;
            const LAS char* kn = kp + dl; const LAS char* k2n = k2p + dl; const LAS char* vn = vp + dl;
            STEP(sa, sb, kp + 2 * 2560, k2p + 2 * 2560, vp + 1 * 2560);
            STEP(sb, sa, kp + 3 * 2560, k2p + 3 * 2560, vp + 2 * 2560);
            STEP(sa, sb, kp + 4 * 2560, k2p + 4 * 2560, vp + 3 * 2560);
            STEP(sb, sa, kp + 5 * 2560, k2p + 5 * 2560, vp + 4 * 2560);
            STEP(sa, sb, kp + 6 * 2560, k2p + 6 * 2560, vp + 5 * 2560);
            STEP(sb, sa, kp + 7 * 2560, k2p + 7 * 2560, vp + 6 * 2560);
            STEP(sa, sb, kn, k2n, vp + 7 * 2560);
            STEP(sb, sa, kn + 2560, k2n + 2560, vn);
            kp = kn; k2p = k2n; vp = vn;
        }
#undef STEP
#undef EX
        lsum = __shfl(o[10], r32, 64);
        bool ok = lsum < 1.0e30f;
#pragma unroll
        for (int r = 0; r < 10; ++r) ok = ok && (fabsf(o[r]) < 3.0e38f);
        if (attempt || __all(ok)) break;
    }
    const float inv = 1.0f / lsum;
    char* of = ws + WS_OF + (size_t)(qrow >> 4) * 72 * 256 + (qrow & 15) * 16;
    *(GAS u32x4*)(of + (2 * head + h) * 256) = u32x4{pk2(o[0] * inv, o[1] * inv), pk2(o[2] * inv, o[3] * inv), pk2(o[4] * inv, o[5] * inv), pk2(o[6] * inv, o[7] * inv)};
    if (h == 0) *(GAS unsigned*)(of + (64 + (head >> 2)) * 256 + (head & 3) * 4) = pk2(o[8] * inv, o[9] * inv);
}

template <int MODE, int NW, int NIT, int RING>
__global__ __launch_bounds__(NW * 64, 1) void k_stream(const u16* __restrict__ x, char* __restrict__ ws, u16* __restrict__ out) {
    constexpr int NK = MODE ? 576 : 72, NPAIR = NK / 8, ROWB = 336;
    static_assert(NW * NIT == NPAIR && NIT % RING == 0, "");
    __shared__ __attribute__((aligned(16))) char lds[NW * 64 * ROWB];
    const int b = blockIdx.x, bi = b >> 3, mb = 4 * (b & 7) + (bi >> 3), nb = bi & 7;
    const int tid = threadIdx.x, l = tid & 63, w = __builtin_amdgcn_readfirstlane(tid >> 6);
    const GAS char* ap = (const GAS char*)ws + (MODE ? WS_AF : WS_OF) + (size_t)mb * 4 * NK * 256 + w * 2048 + l * 16;
    const GAS char* wp = (const GAS char*)ws + (MODE ? WS_WD : WS_WO) + ((size_t)nb * NPAIR + w) * 9216 + l * 16;
    u32x4 af[RING][8], wf[RING][9];
    auto issue = [&](int s, int it) {
#pragma unroll
        for (int c = 0; c < 2; ++c) {
#pragma unroll
            for (int m = 0; m < 4; ++m) af[s][c * 4 + m] = *(const GAS u32x4*)(ap + (size_t)m * NK * 256 + (size_t)it * NW * 2048 + c * 1024);
#pragma unroll
            for (int t = 0; t < 4; ++t) wf[s][c * 4 + t] = *(const GAS u32x4*)(wp + (size_t)it * NW * 9216 + (c * 4 + t) * 1024);
        }
        wf[s][8] = *(const GAS u32x4*)(wp + (size_t)it * NW * 9216 + 8192);
    };
#pragma unroll
    for (int s = 0; s < RING; ++s) issue(s, s);
    SBAR();
    f32x4 acc[6][4];
#pragma unroll
    for (int t = 0; t < 6; ++t)
#pragma unroll
        for (int m = 0; m < 4; ++m) acc[t][m] = f32x4{0, 0, 0, 0};
    for (int it0 = 0; it0 < NIT; it0 += RING) {
#pragma unroll
        for (int s = 0; s < RING; ++s) {
#pragma unroll
            for (int c = 0; c < 2; ++c)
#pragma unroll
                for (int m = 0; m < 4; ++m) {
#pragma unroll
                    for (int t = 0; t < 4; ++t) acc[t][m] = mfma16(wf[s][c * 4 + t], af[s][c * 4 + m], acc[t][m]);
                    acc[4 + c][m] = mfma16(wf[s][8], af[s][c * 4 + m], acc[4 + c][m]);
                }
            SBAR();
            if (NIT > RING) { const int nx = it0 + s + RING; issue(s, nx < NIT ? nx : NIT - 1); }
            SBAR();
        }
    }
    const int i16 = l & 15, q = l >> 4;
    {
        LAS char* dst = (LAS char*)lds + (w * 64 + i16) * ROWB;
#pragma unroll
        for (int m = 0; m < 4; ++m) {
#pragma unroll
            for (int t = 0; t < 4; ++t) *(LAS f32x4*)(dst + m * 16 * ROWB + q * 64 + t * 16) = acc[t][m];
            *(LAS f32x4*)(dst + m * 16 * ROWB + 256 + q * 16) = q < 2 ? acc[4][m] : acc[5][m];
        }
    }
    __syncthreads();
    for (int item = tid; item < 512; item += NW * 64) {
        const int row = item >> 3, c = item & 7;
        const int grow = mb * 64 + row;
        float v[16];
#pragma unroll
        for (int j = 0; j < 16; ++j) v[j] = 0.f;
        const int cc = c < 5 ? c : 4;
#pragma unroll
        for (int ww = 0; ww < NW; ++ww)
#pragma unroll
            for (int t = 0; t < 4; ++t) {
                const f32x4 z = *(const LAS f32x4*)((const LAS char*)lds + (ww * 64 + row) * ROWB + cc * 64 + t * 16);
#pragma unroll
                for (int r = 0; r < 4; ++r) v[t * 4 + r] += z[r];
            }
        float y[16];
#pragma unroll
        for (int j = 0; j < 8; ++j) y[j] = cc < 4 ? v[(j & 3) * 4 + (j >> 2)] : v[j] + v[j + 8];
#pragma unroll
        for (int j = 8; j < 16; ++j) y[j] = v[(j & 3) * 4 + (j >> 2)];
        const int kap = 9 * nb + 2 * cc;
        const char* hf = ws + WS_HF + ((size_t)(grow >> 4) * 72 + kap) * 256 + (grow & 15) * 16;
        const size_t rm = ((size_t)grow * D_ + 8 * kap) * 2;
        u32x4 r0, r1 = {0, 0, 0, 0};
        if (MODE == 0) { r0 = *(const GAS u32x4*)((const char*)x + rm); if (cc < 4) r1 = *(const GAS u32x4*)((const char*)x + rm + 16); }
        else { r0 = *(const GAS u32x4*)hf; if (cc < 4) r1 = *(const GAS u32x4*)(hf + 256); }
        u32x4 o0, o1;
#pragma unroll
        for (int j = 0; j < 4; ++j) {
            o0[j] = pk2(y[2 * j] + bflo(r0[j]), y[2 * j + 1] + bfhi(r0[j]));
            o1[j] = pk2(y[8 + 2 * j] + bflo(r1[j]), y[8 + 2 * j + 1] + bfhi(r1[j]));
        }
        if (MODE == 0) {
            float s1 = 0.f, s2 = 0.f;
#pragma unroll
            for (int j = 0; j < 4; ++j) { s1 = dot2(o0[j], 0x3f803f80u, s1); s2 = dot2(o0[j], o0[j], s2); }
            if (cc < 4) {
#pragma unroll
                for (int j = 0; j < 4; ++j) { s1 = dot2(o1[j], 0x3f803f80u, s1); s2 = dot2(o1[j], o1[j], s2); }
            }
            if (c > 4) { s1 = 0.f; s2 = 0.f; }
#pragma unroll
            for (int d = 1; d < 8; d <<= 1) { s1 += __shfl_xor(s1, d, 64); s2 += __shfl_xor(s2, d, 64); }
            if (c == 0) *(GAS f32x2*)(ws + WS_ST + (size_t)grow * 64 + nb * 8) = f32x2{s1, s2};
            if (c < 5) *(GAS u32x4*)hf = o0;
            if (c < 4) *(GAS u32x4*)(hf + 256) = o1;
        } else {
            if (c < 5) *(GAS u32x4*)((char*)out + rm) = o0;
            if (c < 4) *(GAS u32x4*)((char*)out + rm + 16) = o1;
        }
    }
}

__global__ __launch_bounds__(768, 3) void k4_gateup(char* __restrict__ ws) {
    __shared__ __attribute__((aligned(16))) char lds[147456];
    const int b = blockIdx.x, s = 2 * (b & 7) + ((b >> 3) & 1), mtile = b >> 4;
    const int tid = threadIdx.x, l = tid & 63, w = __builtin_amdgcn_readfirstlane(tid >> 6);
    const int i16 = l & 15, q = l >> 4, W = 12 * s + w;
    const unsigned lds0 = (unsigned)(uintptr_t)lds;
    f32x4 stv[8], csa[3], csb[3];
#pragma unroll
    for (int m = 0; m < 8; ++m) stv[m] = *(const GAS f32x4*)(ws + WS_ST + (size_t)(mtile * 128 + m * 16 + i16) * 64 + q * 16);
#pragma unroll
    for (int t = 0; t < 3; ++t) {
        const char* cp = ws + WS_CS + (size_t)(3 * W + t) * 18 * 64 + q * 16;
        csa[t] = *(const GAS f32x4*)(cp + i16 * 64);
        csb[t] = *(const GAS f32x4*)(cp + (16 + (i16 & 1)) * 64);
    }
    const char* hsrc = ws + WS_HF + (size_t)mtile * 147456 + l * 16;
#pragma unroll
    for (int j = 0; j < 12; ++j) glds16(hsrc + (j * 12 + w) * 1024, lds0 + (j * 12 + w) * 1024);
    const GAS char* wp = (const GAS char*)ws + WS_GU + (size_t)W * 55296 + l * 16;
    u32x4 wf[2][3];
#pragma unroll
    for (int k = 0; k < 2; ++k)
#pragma unroll
        for (int t = 0; t < 3; ++t) wf[k][t] = *(const GAS u32x4*)(wp + (k * 3 + t) * 1024);
    SBAR();
    float rs[8];
    f32x4 acc[3][8];
#pragma unroll
    for (int t = 0; t < 3; ++t)
#pragma unroll
        for (int r = 0; r < 4; ++r) {
            float c = csa[t][r] + (i16 < 2 ? csb[t][r] : 0.f);
#pragma unroll
            for (int d = 1; d < 16; d <<= 1) c += __shfl_xor(c, d, 64);
            csa[t][r] = c;
        }
#pragma unroll
    for (int m = 0; m < 8; ++m) {
        float a = stv[m][0] + stv[m][2], c = stv[m][1] + stv[m][3];
        a += xor16(a); a += xor32(a); c += xor16(c); c += xor32(c);
        const float mean = a * (1.f / D_), var = fmaxf(c * (1.f / D_) - mean * mean, 0.f);
        rs[m] = rsqrtf(var + LN_EPS);
#pragma unroll
        for (int t = 0; t < 3; ++t) acc[t][m] = csa[t] * (-mean);
    }
    WAIT_BAR(0);
    if (w < 4) __builtin_amdgcn_s_setprio(2);
    else if (w < 8) __builtin_amdgcn_s_setprio(1);
    const LAS char* hp = (const LAS char*)lds + l * 16;
    for (int k0 = 0; k0 < 18; k0 += 2) {
#pragma unroll
        for (int k = 0; k < 2; ++k) {
#pragma unroll
            for (int m = 0; m < 8; ++m) {
                const u32x4 hfm = *(const LAS u32x4*)(hp + m * 18432 + k * 1024);
#pragma unroll
                for (int t = 0; t < 3; ++t) acc[t][m] = mfma16(wf[k][t], hfm, acc[t][m]);
            }
            SBAR();
            const int nk = k0 + k + 2 < 18 ? k0 + k + 2 : 17;
#pragma unroll
            for (int t = 0; t < 3; ++t) wf[k][t] = *(const GAS u32x4*)(wp + (nk * 3 + t) * 1024);
            SBAR();
        }
        hp += 2048;
    }
    char* ad = ws + WS_AF + ((size_t)mtile * 8 * 576 + 3 * W) * 256 + l * 16;
    constexpr float K0 = -2.0f * 0.7978845608028654f * 1.4426950408889634f, K1 = K0 * 0.044715f;
#pragma unroll
    for (int m = 0; m < 8; ++m) {
        unsigned d[3];
#pragma unroll
        for (int t = 0; t < 3; ++t) {
            float y[2];
#pragma unroll
            for (int r = 0; r < 2; ++r) {
                const float g = acc[t][m][r] * rs[m], u = acc[t][m][2 + r] * rs[m];
                const float e = __builtin_amdgcn_exp2f(g * __builtin_fmaf(g * g, K1, K0));
                y[r] = g * u * __builtin_amdgcn_rcpf(1.0f + e);
            }
            d[t] = pk2(y[0], y[1]);
        }
        const auto s16 = __builtin_amdgcn_permlane16_swap(d[2], d[2], false, false);
        const auto sA = __builtin_amdgcn_permlane32_swap(d[0], s16[0], false, false);
        const auto sB = __builtin_amdgcn_permlane32_swap(d[1], s16[1], false, false);
        if (l < 48) *(GAS u32x4*)(ad + (size_t)m * 576 * 256) = u32x4{sA[0], sA[1], sB[0], sB[1]};
    }
}

extern "C" void kernel_launch(void* const* d_in, const int* in_sizes, int n_in, void* d_out, int out_size, void* d_ws, size_t ws_size,
                              hipStream_t stream) {
    const u16* x = (const u16*)d_in[0];
    char* ws = (char*)d_ws;
    k1_qkv<<<256, 256, 0, stream>>>(x, (const u16*)d_in[1], ws);
    k2_attn<<<256, 512, 0, stream>>>((const u16*)d_in[2], (const u16*)d_in[4], (const u16*)d_in[5], (const u16*)d_in[6], ws);
    k_stream<0, 3, 3, 3><<<256, 192, 0, stream>>>(x, ws, (u16*)d_out);
    k4_gateup<<<256, 768, 0, stream>>>(ws);
    k_stream<1, 4, 18, 3><<<256, 256, 0, stream>>>(x, ws, (u16*)d_out);
}
```

```cpp
#include <hip/hip_runtime.h>
#include <hip/hip_bf16.h>
#include <stdint.h>

typedef unsigned short u16;
typedef short bf16x8 __attribute__((ext_vector_type(8)));
typedef short bf16x4 __attribute__((ext_vector_type(4)));
typedef float f32x2 __attribute__((ext_vector_type(2)));
typedef float f32x4 __attribute__((ext_vector_type(4)));
typedef float f32x16 __attribute__((ext_vector_type(16)));
typedef unsigned u32x2 __attribute__((ext_vector_type(2)));
typedef unsigned u32x4 __attribute__((ext_vector_type(4)));
typedef __bf16 bf16x2n __attribute__((ext_vector_type(2)));
#define LAS __attribute__((address_space(3)))
#define GAS __attribute__((address_space(1)))
#define DI __device__ __forceinline__
#define SBAR() __builtin_amdgcn_sched_barrier(0)
#define PIN(x) asm volatile("" : "+v"(x))

constexpr int S_ = 2048, D_ = 576, F_ = 4608;
constexpr float LN_EPS = 1e-5f;
constexpr float QSCALE = 0.23570226039551584f * 1.4426950408889634f;

constexpr size_t WS_Q = 0;
constexpr size_t WS_KV = WS_Q + (size_t)S_ * D_ * 2;
constexpr size_t WS_OF = WS_KV + (size_t)32 * 64 * 2560;
constexpr size_t WS_HF = WS_OF + (size_t)S_ * D_ * 2;
constexpr size_t WS_ST = WS_HF + (size_t)S_ * D_ * 2;
constexpr size_t WS_CS = WS_ST + (size_t)S_ * 64;
constexpr size_t WS_WO = WS_CS + (size_t)576 * 18 * 64;
constexpr size_t WS_WD = WS_WO + (size_t)8 * 9 * 9216;
constexpr size_t WS_GU = WS_WD + (size_t)8 * 72 * 9216;
constexpr size_t WS_AF = WS_GU + (size_t)576 * 18 * 1024;

#define STAMP(kid, pt)
DI unsigned pk2(float a, float b) { f32x2 v = {a, b}; return __builtin_bit_cast(unsigned, __builtin_convertvector(v, bf16x2n)); }
DI float bflo(unsigned u) { return __uint_as_float(u << 16); }
DI float bfhi(unsigned u) { return __uint_as_float(u & 0xffff0000u); }
DI float rbf(float a) { return bflo(pk2(a, a)); }
DI f32x4 mfma16(u32x4 a, u32x4 b, f32x4 c) { return __builtin_amdgcn_mfma_f32_16x16x32_bf16(__builtin_bit_cast(bf16x8, a), __builtin_bit_cast(bf16x8, b), c, 0, 0, 0); }
DI f32x16 mfma32(u32x4 a, u32x4 b, f32x16 c) { return __builtin_amdgcn_mfma_f32_32x32x16_bf16(__builtin_bit_cast(bf16x8, a), __builtin_bit_cast(bf16x8, b), c, 0, 0, 0); }
DI f32x16 mfma32k8(u32x2 a, u32x2 b, f32x16 c) { return __builtin_amdgcn_mfma_f32_32x32x8bf16_1k(__builtin_bit_cast(bf16x4, a), __builtin_bit_cast(bf16x4, b), c, 0, 0, 0); }
DI float dot2(unsigned a, unsigned b, float c) { return __builtin_amdgcn_fdot2_f32_bf16(__builtin_bit_cast(bf16x2n, a), __builtin_bit_cast(bf16x2n, b), c, false); }
template <int CTRL> DI float dpp_add(float v) { return v + __builtin_bit_cast(float, __builtin_amdgcn_update_dpp(0, __builtin_bit_cast(int, v), CTRL, 0xf, 0xf, true)); }
constexpr int DPP_X1 = 0xB1, DPP_X2 = 0x4E, DPP_HMIR = 0x141, DPP_ROR4 = 0x124, DPP_ROR8 = 0x128;
DI void glds16(const void* g, unsigned lds_base) {
    unsigned sv;
    asm volatile("s_mov_b32 %0, m0\n\ts_mov_b32 m0, %2\n\ts_nop 0\n\tglobal_load_lds_dwordx4 %1, off\n\ts_mov_b32 m0, %0" : "=&s"(sv) : "v"(g), "s"(lds_base) : "memory");
}
#define WAIT_BAR(N) asm volatile("s_waitcnt vmcnt(" #N ") lgkmcnt(0)\n\ts_barrier" ::: "memory")
#define ALOAD4(dst, ptr) asm volatile("global_load_dwordx4 %0, %1, off" : "=v"(dst) : "v"(ptr) : "memory")
DI int swap23(int v) { return (v & ~12) | ((v & 4) << 1) | ((v & 8) >> 1); }
DI int vpos(int r) {
    const int lo = (int)((0x76543EDCBA983210ull >> (4 * (r & 15))) & 15) + (r == 11 ? 16 : 0);
    return r < 16 ? lo : (r < 19 ? r : 15);
}
constexpr unsigned ONES2 = 0x3f803f80u;

template <bool VT> DI void k1_loop(const LAS char* xa, const LAS char* xb, const GAS char* wp, unsigned (&wr)[6][8], f32x4 (&acc)[2][4]) {
    for (int k0 = 0; k0 < 18; k0 += 6) {
#pragma unroll
        for (int k = 0; k < 6; ++k) {
            u32x4 f0, f1;
#pragma unroll
            for (int a = 0; a < 4; ++a) { f0[a] = __builtin_amdgcn_perm(wr[k][2 * a + 1], wr[k][2 * a], 0x05040100u); f1[a] = __builtin_amdgcn_perm(wr[k][2 * a + 1], wr[k][2 * a], 0x07060302u); }
            SBAR();
            if (k0 < 12) {
#pragma unroll
                for (int j = 0; j < 8; ++j) wr[k][j] = *(const GAS unsigned*)(wp + (size_t)((k + 6) * 32 + j) * 3456);
            }
            SBAR();
#pragma unroll
            for (int m = 0; m < 4; ++m) {
                const u32x4 xf = *(const LAS u32x4*)(((k & 1) ? xb : xa) + m * 18432 + k * 1024);
                acc[0][m] = VT ? mfma16(xf, f0, acc[0][m]) : mfma16(f0, xf, acc[0][m]);
                acc[1][m] = VT ? mfma16(xf, f1, acc[1][m]) : mfma16(f1, xf, acc[1][m]);
            }
        }
        xa += 6144; xb += 6144; wp += (size_t)6 * 32 * 3456;
    }
}

__global__ __launch_bounds__(512, 2) void k1_qkv(const u16* __restrict__ x, const u16* __restrict__ wqkv, char* __restrict__ ws) {
    __shared__ __attribute__((aligned(16))) char lds[73728];
    const int b = blockIdx.x, grp = b & 7, mb = b >> 3;
    const int tid = threadIdx.x, l = tid & 63, w = __builtin_amdgcn_readfirstlane(tid >> 6);
    const int i16 = l & 15, q = l >> 4;
    STAMP(0, 0);
    u32x4 xr[9];
    {
        const GAS char* xp = (const GAS char*)x + (size_t)(mb * 64 + w * 8 + (l >> 3)) * (D_ * 2) + (l & 7) * 16;
#pragma unroll
        for (int n = 0; n < 9; ++n) xr[n] = *(const GAS u32x4*)(xp + n * 128);
    }
    SBAR();
    const int col = w < 6 ? (w >> 1) * 576 + 32 * (w & 1) + 2 * i16 : (w == 6 ? ((i16 >> 2) & 1) * 576 : 1152) + 64 + 2 * (i16 & 3);
    const GAS char* wp = (const GAS char*)wqkv + ((size_t)(8 * q) * 1728 + 72 * grp + col) * 2;
    unsigned wr[6][8];
#pragma unroll
    for (int k = 0; k < 6; ++k)
#pragma unroll
        for (int j = 0; j < 8; ++j) wr[k][j] = *(const GAS unsigned*)(wp + (size_t)(k * 32 + j) * 3456);
    SBAR();
    STAMP(0, 1);
    {
        float s1 = 0.f, s2 = 0.f, t1 = 0.f, t2 = 0.f;
#pragma unroll
        for (int n = 0; n < 9; ++n)
#pragma unroll
            for (int e = 0; e < 4; e += 2) {
                s1 = dot2(xr[n][e], ONES2, s1); s2 = dot2(xr[n][e], xr[n][e], s2);
                t1 = dot2(xr[n][e + 1], ONES2, t1); t2 = dot2(xr[n][e + 1], xr[n][e + 1], t2);
            }
        s1 += t1; s2 += t2;
        s1 = dpp_add<DPP_X1>(s1); s2 = dpp_add<DPP_X1>(s2); s1 = dpp_add<DPP_X2>(s1); s2 = dpp_add<DPP_X2>(s2); s1 = dpp_add<DPP_HMIR>(s1); s2 = dpp_add<DPP_HMIR>(s2);
        const float mean = s1 * (1.f / D_), var = fmaxf(s2 * (1.f / D_) - mean * mean, 0.f);
        const float nm = -rbf(mean), rs = 1.0f / rbf(sqrtf(rbf(rbf(var) + LN_EPS)));
        const int t = w * 8 + (l >> 3);
        const int mt = (t >> 5) * 2 + ((t >> 2) & 1), row = ((t >> 3) & 3) * 4 + (t & 3);
        LAS char* dst = (LAS char*)lds + mt * 18432 + (l & 7) * 256 + ((row ^ (l & 7)) * 16);
#pragma unroll
        for (int n = 0; n < 9; ++n) {
            u32x4 y;
#pragma unroll
            for (int e = 0; e < 4; ++e) {
                const unsigned d = pk2(bflo(xr[n][e]) + nm, bfhi(xr[n][e]) + nm);
                y[e] = pk2(bflo(d) * rs, bfhi(d) * rs);
            }
            *(LAS u32x4*)(dst + n * 2048) = y;
        }
    }
    asm volatile("s_waitcnt lgkmcnt(0)\n\ts_barrier" ::: "memory");
    STAMP(0, 4);
    const LAS char* xa = (const LAS char*)lds + q * 256 + ((i16 ^ q) * 16);
    const LAS char* xb = (const LAS char*)lds + q * 256 + ((i16 ^ (4 + q)) * 16);
    f32x4 acc[2][4];
#pragma unroll
    for (int m = 0; m < 4; ++m) acc[0][m] = acc[1][m] = f32x4{0, 0, 0, 0};
    char* kv0 = ws + WS_KV + ((size_t)(4 * grp) * 64 + mb * 2) * 2560;
    if (w == 4 || w == 5 || w == 7) {
        k1_loop<true>(xa, xb, wp, wr, acc);
        STAMP(0, 2);
#pragma unroll
        for (int t = 0; t < 2; ++t) {
            const int n = col - 1152 + t;
            int hd = n / 18; const int d = n - 18 * hd;
            int pos = vpos(d < 16 ? swap23(d) : d);
            const bool fill = w == 7 && i16 >= 4;
            if (fill) { hd = i16 & 3; pos = t ? 15 : 18; }
#pragma unroll
            for (int tl = 0; tl < 2; ++tl) {
                u32x4 o = {pk2(acc[t][2 * tl][0], acc[t][2 * tl][1]), pk2(acc[t][2 * tl][2], acc[t][2 * tl][3]),
                           pk2(acc[t][2 * tl + 1][0], acc[t][2 * tl + 1][1]), pk2(acc[t][2 * tl + 1][2], acc[t][2 * tl + 1][3])};
                if (fill) { const unsigned f = t ? 0u : ONES2; o = u32x4{f, f, f, f}; }
                *(GAS u32x4*)(kv0 + (size_t)hd * 163840 + tl * 2560 + 1280 + q * 320 + pos * 16) = o;
            }
        }
    } else {
        k1_loop<false>(xa, xb, wp, wr, acc);
        STAMP(0, 2);
        const int c = w == 6 ? 8 : 4 * (w & 1) + q;
        const bool isq = w == 6 ? !(q & 1) : w < 2;
        const int cm = c > 4 ? c - 1 : c;
#pragma unroll
        for (int m = 0; m < 4; ++m) {
            u32x4 o;
#pragma unroll
            for (int r = 0; r < 4; ++r) { o[r] = pk2(acc[0][m][r], acc[1][m][r]); if (isq) o[r] = pk2(bflo(o[r]) * QSCALE, bfhi(o[r]) * QSCALE); }
            const int t5 = 8 * (i16 >> 2) + 4 * (m & 1) + (i16 & 3), rho = swap23(t5);
            char* kt = kv0 + (m >> 1) * 2560;
            char* dst = isq ? ws + WS_Q + ((size_t)(grp * 9 + c) * 2048 + mb * 64 + (m >> 1) * 32 + t5) * 16
                            : kt + (size_t)(cm >> 1) * 163840 + (cm & 1) * 512 + rho * 16;
            if (isq || c != 4) *(GAS u32x4*)dst = o;
            if (!isq && (c == 2 || c == 4)) *(GAS u32x2*)(kt + (size_t)(c / 2 - 1) * 163840 + 1024 + rho * 8) = u32x2{o[0], o[1]};
            if (!isq && (c == 4 || c == 6)) *(GAS u32x2*)(kt + (size_t)(c / 2) * 163840 + 1024 + rho * 8) = u32x2{o[2], o[3]};
        }
    }
    STAMP(0, 3);
}

DI void wd_rows(int kap, int& A, int& B) {
    const int W = kap / 3, qq = kap - 3 * W, T0 = 3 * W + (qq == 2 ? 2 : 0), T1 = 3 * W + (qq == 2 ? 2 : 1);
    A = 64 * (T0 >> 3) + (T0 & 7) + (qq == 2 ? 0 : 16 * qq);
    B = 64 * (T1 >> 3) + (T1 & 7) + (qq == 2 ? 16 : 16 * qq);
}

__global__ __launch_bounds__(512, 2) void k2_attn(const u16* __restrict__ wo, const u16* __restrict__ wg, const u16* __restrict__ wu,
                                                  const u16* __restrict__ wd, char* __restrict__ ws) {
    __shared__ __attribute__((aligned(16))) char lds[163840];
    const int b = blockIdx.x, xcd = b & 7, bi = b >> 3, hh = bi & 3, head = 4 * xcd + hh, qb = bi >> 2;
    const int tid = threadIdx.x, l = tid & 63, w = __builtin_amdgcn_readfirstlane(tid >> 6);
    const int r32 = l & 31, h = l >> 5;
    const unsigned lds0 = (unsigned)(uintptr_t)lds;
    const int qrow = qb * 256 + w * 32 + r32;
    STAMP(1, 0);
    const char* qp = ws + WS_Q + ((size_t)xcd * 9 * 2048 + qrow) * 16;
    u32x4 q1 = *(const GAS u32x4*)(qp + (size_t)(2 * hh + (hh >> 1) + h) * 32768);
    u32x2 q2 = *(const GAS u32x2*)(qp + (size_t)(2 + 2 * ((hh + 1) >> 1)) * 32768 + (hh >> 1) * 8);
    const char* kvsrc = ws + WS_KV + (size_t)head * 163840 + w * 1024 + l * 16;
#pragma unroll
    for (int j = 0; j < 6; ++j) glds16(kvsrc + j * 8192, lds0 + w * 1024 + j * 8192);
    const int v = bi * 8 + w;
    const int kq = l >> 4, i16 = l & 15, i8 = l & 7, par = i16 >> 3;
    const char* spA; const char* spB; int s0, s1, lof;
    char* sdst; int dlo, dhi;
    const int kind = v < 162 ? 0 : (v < 243 ? 1 : 2);
    if (kind == 0) {
        const int u = 9 * xcd + v / 18, ks = v % 18;
        spA = (const char*)(par ? wu : wg) + ((32 * ks + 8 * kq) * F_ + 64 * u + 8 * i8) * 2;
        spB = spA + 4 * F_ * 2; s0 = F_ * 2; s1 = 2 * F_ * 2; lof = 0;
        sdst = ws + WS_GU + ((size_t)(8 * u) * 18 + ks) * 1024 + kq * 256 + (4 * (i8 >> 1) + 2 * par + (i8 & 1)) * 16;
        dlo = 18432; dhi = 4 * 18432;
    } else {
        const int idx = kind == 1 ? xcd * 81 + (v - 162) : 0;
        const bool isd = idx < 576; const int id2 = isd ? idx : idx - 576, pr = id2 >> 3, nb = id2 & 7, kap = 8 * pr + 4 * par + kq;
        int A, B;
        wd_rows(kap, A, B);
        if (!isd) { A = pr < 8 ? 18 * (kap >> 1) + 8 * (kap & 1) : 72 * (kap - 64) + 16; B = A + (pr < 8 ? 4 : 36); }
        const char* bp = (const char*)(isd ? wd : wo) + (72 * nb + 8 * i8) * 2;
        spA = bp + A * (D_ * 2); spB = bp + B * (D_ * 2);
        s0 = (isd ? 8 : 1) * D_ * 2; s1 = (isd ? 32 : (pr < 8 ? 2 : 18)) * D_ * 2; lof = 128 - 14 * i8;
        sdst = ws + (isd ? WS_WD : WS_WO) + ((size_t)nb * (isd ? 72 : 9) + pr) * 9216 + par * 4096 + kq * 256 + i8 * 16;
        dlo = 1024; dhi = 128;
    }
    if (hh == 1 && h == 0) q1[0] = 0;
    if (hh == 2 && h == 1) q1[3] = 0;
    if (hh == 3 || h) q2[0] = 0;
    if (hh == 0 || h) q2[1] = 0;
    PIN(q1); PIN(q2);
    WAIT_BAR(0);
    STAMP(1, 1);
    u32x4 sd[8], sl = {0, 0, 0, 0}, sh = {0, 0, 0, 0};
#pragma unroll
    for (int j = 0; j < 8; ++j) ALOAD4(sd[j], (j < 4 ? spA : spB) + ((j >> 1) & 1) * s1 + (j & 1) * s0);
    if (kind == 1) {
#pragma unroll
        for (int j = 0; j < 8; j += 2) {
            const char* p = (j < 4 ? spA : spB) + ((j >> 1) & 1) * s1 + lof;
            asm volatile("global_load_ushort %0, %2, off\n\tglobal_load_ushort %1, %3, off" : "+v"(sl[j >> 1]), "+v"(sh[j >> 1]) : "v"(p), "v"(p + s0) : "memory");
        }
    }
#pragma unroll
    for (int j = 6; j < 20; ++j) glds16(kvsrc + j * 8192, lds0 + w * 1024 + j * 8192);
    SBAR();

    const LAS char* const kp0 = (const LAS char*)lds + h * 512 + r32 * 16;
    const LAS char* const k2p0 = (const LAS char*)lds + 1024 + r32 * 8;
    const LAS char* const vp0 = (const LAS char*)lds + 1280 + h * 320 + vpos(r32) * 16;
    f32x16 o;
    float lsum;
    for (int attempt = 0;; ++attempt) {
        float mx = -3.0e38f;
        const int nt = attempt ? 64 : 1;
        for (int t = 0; t < nt; ++t) {
            f32x16 s = mfma32k8(*(const LAS u32x2*)(k2p0 + t * 2560), q2, f32x16{});
            s = mfma32(*(const LAS u32x4*)(kp0 + t * 2560), q1, s);
#pragma unroll
            for (int r = 0; r < 16; ++r) mx = fmaxf(mx, s[r]);
        }
        mx = fmaxf(mx, __shfl_xor(mx, 32, 64));
        f32x16 negm;
#pragma unroll
        for (int r = 0; r < 16; ++r) negm[r] = -mx;
        PIN(negm);
        o = f32x16{};
        const LAS char* kp = kp0; const LAS char* k2p = k2p0; const LAS char* vp = vp0;
        f32x16 sa, sb;
        sa = mfma32(*(const LAS u32x4*)kp, q1, mfma32k8(*(const LAS u32x2*)k2p, q2, negm));
        u32x4 kf = *(const LAS u32x4*)(kp + 2560); u32x2 k2 = *(const LAS u32x2*)(k2p + 2560);
        u32x4 v0 = *(const LAS u32x4*)vp, v1 = *(const LAS u32x4*)(vp + 640);
#define EX(x) __builtin_amdgcn_exp2f(x)
#define STEP(C, N, KP, K2P, VP) do { u32x4 p0, p1; \
            N = mfma32k8(k2, q2, negm); k2 = *(const LAS u32x2*)(K2P); \
            C[0] = EX(C[0]); C[1] = EX(C[1]); C[2] = EX(C[2]); C[3] = EX(C[3]); p0[0] = pk2(C[0], C[1]); p0[1] = pk2(C[2], C[3]); PIN(p0[0]); PIN(p0[1]); SBAR(); \
            N = mfma32(kf, q1, N); kf = *(const LAS u32x4*)(KP); \
            C[4] = EX(C[4]); C[5] = EX(C[5]); C[6] = EX(C[6]); C[7] = EX(C[7]); p0[2] = pk2(C[4], C[5]); p0[3] = pk2(C[6], C[7]); \
            C[8] = EX(C[8]); C[9] = EX(C[9]); PIN(p0); PIN(C[8]); PIN(C[9]); SBAR(); \
            o = mfma32(v0, p0, o); v0 = *(const LAS u32x4*)(VP); \
            C[10] = EX(C[10]); C[11] = EX(C[11]); C[12] = EX(C[12]); C[13] = EX(C[13]); C[14] = EX(C[14]); C[15] = EX(C[15]); \
            p1[0] = pk2(C[8], C[9]); p1[1] = pk2(C[10], C[11]); p1[2] = pk2(C[12], C[13]); p1[3] = pk2(C[14], C[15]); PIN(p1); SBAR(); \
            o = mfma32(v1, p1, o); v1 = *(const LAS u32x4*)((VP) + 640); SBAR(); } while (0)
        for (int it = 0; it < 8; ++it) {
            if (attempt == 0 && it == 2) {
                STAMP(1, 4);
                asm volatile("s_waitcnt vmcnt(0) lgkmcnt(0)\n\ts_barrier"
                             : "+v"(sd[0]), "+v"(sd[1]), "+v"(sd[2]), "+v"(sd[3]), "+v"(sd[4]), "+v"(sd[5]), "+v"(sd[6]), "+v"(sd[7]), "+v"(sl), "+v"(sh), "+v"(sdst) :: "memory");
                STAMP(1, 5);
                if (kind != 2) {
                    f32x4 csum = {0, 0, 0, 0};
#pragma unroll
                    for (int t = 0; t < 8; ++t) {
                        u32x4 f;
#pragma unroll
                        for (int a = 0; a < 4; ++a) f[a] = __builtin_amdgcn_perm(sd[2 * a + 1][t >> 1], sd[2 * a][t >> 1], (t & 1) ? 0x07060302u : 0x05040100u);
                        char* dp = sdst + (t & 3) * dlo + (t >> 2) * dhi;
                        asm volatile("global_store_dwordx4 %0, %1, off sc1\n\ts_nop 1" :: "v"(dp), "v"(f) : "memory");
                        const unsigned sel = i16 == t ? ONES2 : 0u;
                        csum = mfma16(f, u32x4{sel, sel, sel, sel}, csum);
                    }
                    if (kind == 0) {
                        if (i16 < 8) {
                            float* cp = (float*)(ws + WS_CS) + ((size_t)(8 * (9 * xcd + v / 18) + i16) * 4 + 2 * (kq & 1)) * 72 + (v % 18) * 4 + (kq >> 1) * 2;
                            *(GAS f32x2*)cp = f32x2{csum[0], csum[1]};
                            *(GAS f32x2*)(cp + 72) = f32x2{csum[2], csum[3]};
                        }
                    } else *(GAS u32x4*)(sdst + 8192 - par * 3968) = sl | (sh << 16);
                }
                STAMP(1, 6);
            }
            const int dl = it == 7 ? -56 * 2560 : 8 * 2560;
            const LAS char* kn = kp + dl; const LAS char* k2n = k2p + dl; const LAS char* vn = vp + dl;
            STEP(sa, sb, kp + 2 * 2560, k2p + 2 * 2560, vp + 1 * 2560);
            STEP(sb, sa, kp + 3 * 2560, k2p + 3 * 2560, vp + 2 * 2560);
            STEP(sa, sb, kp + 4 * 2560, k2p + 4 * 2560, vp + 3 * 2560);
            STEP(sb, sa, kp + 5 * 2560, k2p + 5 * 2560, vp + 4 * 2560);
            STEP(sa, sb, kp + 6 * 2560, k2p + 6 * 2560, vp + 5 * 2560);
            STEP(sb, sa, kp + 7 * 2560, k2p + 7 * 2560, vp + 6 * 2560);
            STEP(sa, sb, kn, k2n, vp + 7 * 2560);
            STEP(sb, sa, kn + 2560, k2n + 2560, vn);
            kp = kn; k2p = k2n; vp = vn;
        }
#undef STEP
#undef EX
        lsum = __shfl(o[10], r32, 64);
        bool ok = lsum < 1.0e30f;
#pragma unroll
        for (int r = 0; r < 10; ++r) ok = ok && (fabsf(o[r]) < 3.0e38f);
        if (attempt || __all(ok)) break;
    }
    STAMP(1, 2);
    const float inv = 1.0f / lsum;
    char* of = ws + WS_OF + (size_t)(qrow >> 4) * 72 * 256 + (qrow & 15) * 16;
    *(GAS u32x4*)(of + (2 * head + h) * 256) = u32x4{pk2(o[0] * inv, o[1] * inv), pk2(o[2] * inv, o[3] * inv), pk2(o[4] * inv, o[5] * inv), pk2(o[6] * inv, o[7] * inv)};
    if (h == 0) *(GAS unsigned*)(of + (64 + (head >> 2)) * 256 + (head & 3) * 4) = pk2(o[8] * inv, o[9] * inv);
    STAMP(1, 3);
}

template <int MODE, int NW, int NIT, int RING>
__global__ __launch_bounds__(256, 1) void k_stream(const u16* __restrict__ x, char* __restrict__ ws, u16* __restrict__ out) {
    constexpr int NK = MODE ? 576 : 72, NPAIR = NK / 8, ROWB = 336;
    static_assert(NW * NIT == NPAIR && NIT % RING == 0, "");
    __shared__ __attribute__((aligned(16))) char lds[NW * 64 * ROWB];
    const int b = blockIdx.x, bi = b >> 3, mb = 4 * (b & 7) + (bi >> 3), nb = bi & 7;
    const int tid = threadIdx.x, l = tid & 63, w = __builtin_amdgcn_readfirstlane(tid >> 6);
    STAMP(2 + 2 * MODE, 0);
    const int row = tid >> 2, j4 = tid & 3, grow = mb * 64 + row;
    char* hf = ws + WS_HF + ((size_t)(grow >> 4) * 72 + 9 * nb) * 256 + (grow & 15) * 16;
    const size_t rm = ((size_t)grow * D_ + 72 * nb) * 2;
    u32x4 r0, r1; unsigned r2;
    if (MODE == 0) { const char* xp = (const char*)x + rm; r0 = *(const GAS u32x4*)(xp + j4 * 32); r1 = *(const GAS u32x4*)(xp + j4 * 32 + 16); r2 = *(const GAS unsigned*)(xp + 128 + j4 * 4); }
    else { r0 = *(const GAS u32x4*)(hf + j4 * 512); r1 = *(const GAS u32x4*)(hf + j4 * 512 + 256); r2 = *(const GAS unsigned*)(hf + 2048 + j4 * 4); }
    SBAR();
    if (NW == 4 || w < NW) {
        const GAS char* ap = (const GAS char*)ws + (MODE ? WS_AF : WS_OF) + (size_t)mb * 4 * NK * 256 + w * 2048 + l * 16;
        const GAS char* wp = (const GAS char*)ws + (MODE ? WS_WD : WS_WO) + ((size_t)nb * NPAIR + w) * 9216 + l * 16;
        u32x4 af[RING][8], wf[RING][9];
        auto issue = [&](int s, int it) {
            wf[s][8] = *(const GAS u32x4*)(wp + (size_t)it * NW * 9216 + 8192);
#pragma unroll
            for (int c = 0; c < 2; ++c) {
#pragma unroll
                for (int t = 0; t < 4; ++t) wf[s][c * 4 + t] = *(const GAS u32x4*)(wp + (size_t)it * NW * 9216 + (c * 4 + t) * 1024);
#pragma unroll
                for (int m = 0; m < 4; ++m) af[s][c * 4 + m] = *(const GAS u32x4*)(ap + (size_t)m * NK * 256 + (size_t)it * NW * 2048 + c * 1024);
            }
        };
#pragma unroll
        for (int s = 0; s < RING; ++s) issue(s, s);
        SBAR();
        STAMP(2 + 2 * MODE, 1);
        f32x4 acc[6][4];
#pragma unroll
        for (int t = 0; t < 6; ++t)
#pragma unroll
            for (int m = 0; m < 4; ++m) acc[t][m] = f32x4{0, 0, 0, 0};
#pragma unroll 1
        for (int it0 = 0; it0 < NIT; it0 += RING) {
#pragma unroll
            for (int s = 0; s < RING; ++s) {
#pragma unroll
                for (int c = 0; c < 2; ++c)
#pragma unroll
                    for (int m = 0; m < 4; ++m) {
#pragma unroll
                        for (int t = 0; t < 4; ++t) acc[t][m] = mfma16(wf[s][c * 4 + t], af[s][c * 4 + m], acc[t][m]);
                        acc[4 + c][m] = mfma16(wf[s][8], af[s][c * 4 + m], acc[4 + c][m]);
                    }
                SBAR();
                if (it0 + RING < NIT) issue(s, it0 + s + RING);
                SBAR();
            }
        }
        STAMP(2 + 2 * MODE, 2);
        const int i16 = l & 15, q = l >> 4;
        LAS char* dst = (LAS char*)lds + (w * 64 + i16) * ROWB;
#pragma unroll
        for (int m = 0; m < 4; ++m) {
#pragma unroll
            for (int t = 0; t < 4; ++t) *(LAS f32x4*)(dst + m * 16 * ROWB + (q & 1) * 128 + (q >> 1) * 64 + t * 16) = acc[t][m];
            *(LAS f32x4*)(dst + m * 16 * ROWB + 256 + q * 16) = q < 2 ? acc[4][m] : acc[5][m];
        }
    }
    __syncthreads();
    STAMP(2 + 2 * MODE, 4);
    f32x2 v[8], vl = {0.f, 0.f};
#pragma unroll
    for (int e = 0; e < 8; ++e) v[e] = f32x2{0.f, 0.f};
#pragma unroll
    for (int ww = 0; ww < NW; ++ww) {
        const LAS char* src = (const LAS char*)lds + (ww * 64 + row) * ROWB;
#pragma unroll
        for (int e = 0; e < 8; ++e) v[e] += *(const LAS f32x2*)(src + (j4 >> 1) * 128 + e * 16 + (j4 & 1) * 8);
        vl += *(const LAS f32x2*)(src + 256 + j4 * 8);
        vl += *(const LAS f32x2*)(src + 288 + j4 * 8);
    }
    u32x4 o0, o1;
#pragma unroll
    for (int j = 0; j < 4; ++j) {
        o0[j] = pk2(v[2 * j][0] + bflo(r0[j]), v[2 * j + 1][0] + bfhi(r0[j]));
        o1[j] = pk2(v[2 * j][1] + bflo(r1[j]), v[2 * j + 1][1] + bfhi(r1[j]));
    }
    const unsigned o2 = pk2(vl[0] + bflo(r2), vl[1] + bfhi(r2));
    if (MODE == 0) {
        *(GAS u32x4*)(hf + j4 * 512) = o0; *(GAS u32x4*)(hf + j4 * 512 + 256) = o1; *(GAS unsigned*)(hf + 2048 + j4 * 4) = o2;
        float s1 = dot2(o2, ONES2, 0.f), s2 = dot2(o2, o2, 0.f);
#pragma unroll
        for (int j = 0; j < 4; ++j) { s1 = dot2(o0[j], ONES2, s1); s2 = dot2(o0[j], o0[j], s2); s1 = dot2(o1[j], ONES2, s1); s2 = dot2(o1[j], o1[j], s2); }
        s1 = dpp_add<DPP_X1>(s1); s2 = dpp_add<DPP_X1>(s2); s1 = dpp_add<DPP_X2>(s1); s2 = dpp_add<DPP_X2>(s2);
        if (j4 == 0) *(GAS f32x2*)(ws + WS_ST + (size_t)grow * 64 + nb * 8) = f32x2{s1, s2};
    } else {
        char* op = (char*)out + rm;
        *(GAS u32x4*)(op + j4 * 32) = o0; *(GAS u32x4*)(op + j4 * 32 + 16) = o1; *(GAS unsigned*)(op + 128 + j4 * 4) = o2;
    }
    STAMP(2 + 2 * MODE, 3);
}

__global__ __launch_bounds__(768, 3) void k4_gateup(char* __restrict__ ws) {
    __shared__ __attribute__((aligned(16))) char lds[147456 + 3072];
    const int b = blockIdx.x, s = 2 * (b & 7) + ((b >> 3) & 1), mtile = b >> 4;
    const int tid = threadIdx.x, l = tid & 63, w = __builtin_amdgcn_readfirstlane(tid >> 6);
    const int i16 = l & 15, q = l >> 4, W = 12 * s + w;
    const unsigned lds0 = (unsigned)(uintptr_t)lds;
    STAMP(3, 0);
    f32x4 stv, csa[3], csb[3];
    ALOAD4(stv, ws + WS_ST + (size_t)mtile * 8192 + (tid & 511) * 16);
#pragma unroll
    for (int t = 0; t < 3; ++t) {
        const char* cp = ws + WS_CS + ((size_t)(3 * W + t) * 4 + q) * 288;
        ALOAD4(csa[t], cp + i16 * 16);
        ALOAD4(csb[t], cp + 256 + (i16 & 1) * 16);
    }
    const char* hsrc = ws + WS_HF + (size_t)mtile * 147456 + l * 16;
    auto hpiece = [&](int j) { const int idx = j * 12 + w; glds16(hsrc + ((idx & 7) * 18 + (idx >> 3)) * 1024, lds0 + idx * 1024); };
#pragma unroll
    for (int j = 0; j < 3; ++j) hpiece(j);
    const char* wp = ws + WS_GU + (size_t)W * 55296 + l * 16;
    u32x4 wf[3][3];
#pragma unroll
    for (int k = 0; k < 3; ++k)
#pragma unroll
        for (int t = 0; t < 3; ++t) ALOAD4(wf[k][t], wp + t * 18432 + k * 1024);
#pragma unroll
    for (int j = 3; j < 12; ++j) hpiece(j);
    STAMP(3, 4);
    asm volatile("s_waitcnt vmcnt(21)" : "+v"(stv), "+v"(csa[0]), "+v"(csa[1]), "+v"(csa[2]), "+v"(csb[0]), "+v"(csb[1]), "+v"(csb[2]));
    LAS f32x2* st = (LAS f32x2*)(lds + 147456);
    {
        float a = stv[0] + stv[2], c = stv[1] + stv[3];
        a = dpp_add<DPP_X1>(a); c = dpp_add<DPP_X1>(c); a = dpp_add<DPP_X2>(a); c = dpp_add<DPP_X2>(c);
        const float mean = a * (1.f / D_), var = fmaxf(c * (1.f / D_) - mean * mean, 0.f);
        st[(tid & 511) >> 2] = f32x2{__builtin_amdgcn_rsqf(var + LN_EPS), -mean};
    }
#pragma unroll
    for (int t = 0; t < 3; ++t)
#pragma unroll
        for (int r = 0; r < 4; ++r) {
            float c = csa[t][r] + (i16 < 2 ? csb[t][r] : 0.f);
            c = dpp_add<DPP_X1>(c); c = dpp_add<DPP_X2>(c); c = dpp_add<DPP_ROR4>(c); c = dpp_add<DPP_ROR8>(c);
            csa[t][r] = c;
        }
    asm volatile("s_waitcnt lgkmcnt(0)\n\ts_barrier" ::: "memory");
    f32x4 acc[3][8];
#pragma unroll
    for (int m = 0; m < 8; ++m) {
        const float nm = st[m * 16 + i16][1];
#pragma unroll
        for (int t = 0; t < 3; ++t) { acc[t][m] = csa[t] * nm; PIN(acc[t][m]); }
    }
    STAMP(3, 5);
    WAIT_BAR(15);
    STAMP(3, 1);
    const LAS char* hp = (const LAS char*)lds + l * 16;
    u32x4 hb[4];
#pragma unroll
    for (int n = 0; n < 3; ++n) hb[n] = *(const LAS u32x4*)(hp + n * 1024);
#pragma unroll 1
    for (int k0 = 0; k0 < 18; k0 += 3) {
        if (k0 == 3) {
            asm volatile("s_waitcnt vmcnt(6)\n\ts_barrier" ::: "memory");
            if (w < 4) __builtin_amdgcn_s_setprio(3); else if (w < 8) __builtin_amdgcn_s_setprio(2); else __builtin_amdgcn_s_setprio(1);
        }
        wp += 3072;
#pragma unroll
        for (int k = 0; k < 3; ++k) {
            if (k0 == 15 && k > 0) asm volatile("s_waitcnt vmcnt(0)" ::: "memory");
            else if (k0 > 0) asm volatile("s_waitcnt vmcnt(6)" ::: "memory");
            asm volatile("s_waitcnt vmcnt(15)" : "+v"(wf[k][0]), "+v"(wf[k][1]), "+v"(wf[k][2]));
#pragma unroll
            for (int m = 0; m < 8; ++m) {
                const int n = k * 8 + m;
                hb[(n + 3) & 3] = *(const LAS u32x4*)(hp + (n + 3) * 1024);
#pragma unroll
                for (int t = 0; t < 3; ++t) acc[t][m] = mfma16(wf[k][t], hb[n & 3], acc[t][m]);
                SBAR();
            }
            if (k0 < 15) {
#pragma unroll
                for (int t = 0; t < 3; ++t) asm volatile("global_load_dwordx4 %0, %1, off" : "+v"(wf[k][t]) : "v"(wp + t * 18432 + k * 1024) : "memory");
            }
            SBAR();
        }
        hp += 24576;
    }
    __builtin_amdgcn_s_setprio(0);
    STAMP(3, 2);
    constexpr float K0 = -2.0f * 0.7978845608028654f * 1.4426950408889634f, K1 = K0 * 0.044715f;
    u32x4 res[8];
#pragma unroll
    for (int m = 0; m < 8; ++m) {
        const float rs = st[m * 16 + i16][0];
        unsigned d[3];
#pragma unroll
        for (int t = 0; t < 3; ++t) {
            float y[2];
#pragma unroll
            for (int r = 0; r < 2; ++r) {
                const float g = acc[t][m][r] * rs, u = acc[t][m][2 + r] * rs;
                const float e = __builtin_amdgcn_exp2f(g * __builtin_fmaf(g * g, K1, K0));
                y[r] = g * u * __builtin_amdgcn_rcpf(1.0f + e);
            }
            d[t] = pk2(y[0], y[1]);
        }
        const auto s16 = __builtin_amdgcn_permlane16_swap(d[2], d[2], false, false);
        const auto sA = __builtin_amdgcn_permlane32_swap(d[0], s16[0], false, false);
        const auto sB = __builtin_amdgcn_permlane32_swap(d[1], s16[1], false, false);
        res[m] = u32x4{sA[0], sA[1], sB[0], sB[1]};
    }
    char* ad = ws + WS_AF + ((size_t)mtile * 8 * 576 + 3 * W) * 256 + l * 16;
    if (l < 48) {
#pragma unroll
        for (int m = 0; m < 8; ++m) *(GAS u32x4*)(ad + (size_t)m * 576 * 256) = res[m];
    }
    STAMP(3, 3);
}

extern "C" void kernel_launch(void* const* d_in, const int* in_sizes, int n_in, void* d_out, int out_size, void* d_ws, size_t ws_size,
                              hipStream_t stream) {
    const u16* x = (const u16*)d_in[0];
    char* ws = (char*)d_ws;
    k1_qkv<<<256, 512, 0, stream>>>(x, (const u16*)d_in[1], ws);
    k2_attn<<<256, 512, 0, stream>>>((const u16*)d_in[2], (const u16*)d_in[4], (const u16*)d_in[5], (const u16*)d_in[6], ws);
    k_stream<0, 3, 3, 3><<<256, 256, 0, stream>>>(x, ws, (u16*)d_out);
    k4_gateup<<<256, 768, 0, stream>>>(ws);
    k_stream<1, 4, 18, 3><<<256, 256, 0, stream>>>(x, ws, (u16*)d_out);
}
```

```cpp
#include <hip/hip_runtime.h>
#include <hip/hip_bf16.h>
#include <stdint.h>

typedef unsigned short u16;
typedef short bf16x8 __attribute__((ext_vector_type(8)));
typedef short bf16x4 __attribute__((ext_vector_type(4)));
typedef float f32x2 __attribute__((ext_vector_type(2)));
typedef float f32x4 __attribute__((ext_vector_type(4)));
typedef float f32x16 __attribute__((ext_vector_type(16)));
typedef unsigned u32x2 __attribute__((ext_vector_type(2)));
typedef unsigned u32x4 __attribute__((ext_vector_type(4)));
typedef __bf16 bf16x2n __attribute__((ext_vector_type(2)));
#define LAS __attribute__((address_space(3)))
#define GAS __attribute__((address_space(1)))
#define DI __device__ __forceinline__
#define SBAR() __builtin_amdgcn_sched_barrier(0)
#define PIN(x) asm volatile("" : "+v"(x))

#define DSL 120
#define PFN 1
constexpr int S_ = 2048, D_ = 576, F_ = 4608;
constexpr float LN_EPS = 1e-5f;
constexpr float QSCALE = 0.23570226039551584f * 1.4426950408889634f;

constexpr size_t WS_Q = 0;
constexpr size_t WS_KV = WS_Q + (size_t)S_ * D_ * 2;
constexpr size_t WS_OF = WS_KV + (size_t)32 * 64 * 2560;
constexpr size_t WS_HF = WS_OF + (size_t)S_ * D_ * 2;
constexpr size_t WS_HC = WS_HF + (size_t)S_ * D_ * 2;
constexpr size_t WS_ST = WS_HC + (size_t)S_ * D_ * 2;
constexpr size_t WS_XM = WS_ST + (size_t)S_ * 32;
constexpr size_t WS_WB = WS_XM + (size_t)S_ * 4;
constexpr size_t WS_WO = WS_WB + (size_t)3072;
constexpr size_t WS_WD = WS_WO + (size_t)8 * 9 * 9216;
constexpr size_t WS_GU = WS_WD + (size_t)8 * 72 * 9216;
constexpr size_t WS_AF = WS_GU + (size_t)576 * 18 * 1024;
constexpr size_t WS_EX = WS_AF + (size_t)S_ * F_ * 2;
constexpr size_t WS_CT = WS_EX + (size_t)64 * 16 * 9216;

#define STAMP(kid, pt)
DI unsigned pk2(float a, float b) { f32x2 v = {a, b}; return __builtin_bit_cast(unsigned, __builtin_convertvector(v, bf16x2n)); }
DI float bflo(unsigned u) { return __uint_as_float(u << 16); }
DI float bfhi(unsigned u) { return __uint_as_float(u & 0xffff0000u); }
DI float rbf(float a) { return bflo(pk2(a, a)); }
DI f32x4 mfma16(u32x4 a, u32x4 b, f32x4 c) { return __builtin_amdgcn_mfma_f32_16x16x32_bf16(__builtin_bit_cast(bf16x8, a), __builtin_bit_cast(bf16x8, b), c, 0, 0, 0); }
DI f32x16 mfma32(u32x4 a, u32x4 b, f32x16 c) { return __builtin_amdgcn_mfma_f32_32x32x16_bf16(__builtin_bit_cast(bf16x8, a), __builtin_bit_cast(bf16x8, b), c, 0, 0, 0); }
DI f32x16 mfma32k8(u32x2 a, u32x2 b, f32x16 c) { return __builtin_amdgcn_mfma_f32_32x32x8bf16_1k(__builtin_bit_cast(bf16x4, a), __builtin_bit_cast(bf16x4, b), c, 0, 0, 0); }
DI float dot2(unsigned a, unsigned b, float c) { return __builtin_amdgcn_fdot2_f32_bf16(__builtin_bit_cast(bf16x2n, a), __builtin_bit_cast(bf16x2n, b), c, false); }
template <int CTRL> DI float dpp_add(float v) { return v + __builtin_bit_cast(float, __builtin_amdgcn_update_dpp(0, __builtin_bit_cast(int, v), CTRL, 0xf, 0xf, true)); }
constexpr int DPP_X1 = 0xB1, DPP_X2 = 0x4E, DPP_HMIR = 0x141;
#define GL_(o, x) "\n\tglobal_load_lds_dwordx4 %1, %2 offset:" #o x
#define GLDS_N(NAME, X) template <int N> DI void NAME(const void* sbase, unsigned voff, unsigned lds_base) { unsigned sv; \
    if (N == 1) asm volatile("s_mov_b32 %0, m0\n\ts_mov_b32 m0, %3\n\ts_nop 0" GL_(0, X) "\n\ts_mov_b32 m0, %0" : "=&s"(sv) : "v"(voff), "s"(sbase), "s"(lds_base) : "memory"); \
    if (N == 2) asm volatile("s_mov_b32 %0, m0\n\ts_mov_b32 m0, %3\n\ts_nop 0" GL_(0, X) GL_(1024, X) "\n\ts_mov_b32 m0, %0" : "=&s"(sv) : "v"(voff), "s"(sbase), "s"(lds_base) : "memory"); \
    if (N == 3) asm volatile("s_mov_b32 %0, m0\n\ts_mov_b32 m0, %3\n\ts_nop 0" GL_(0, X) GL_(1024, X) GL_(2048, X) "\n\ts_mov_b32 m0, %0" : "=&s"(sv) : "v"(voff), "s"(sbase), "s"(lds_base) : "memory"); \
    if (N == 4) asm volatile("s_mov_b32 %0, m0\n\ts_mov_b32 m0, %3\n\ts_nop 0" GL_(0, X) GL_(1024, X) GL_(2048, X) GL_(3072, X) "\n\ts_mov_b32 m0, %0" : "=&s"(sv) : "v"(voff), "s"(sbase), "s"(lds_base) : "memory"); }
GLDS_N(gldsn, "")
GLDS_N(gldsn_dev, " sc1")
#define LBAR() do { asm volatile("" ::: "memory"); __builtin_amdgcn_s_waitcnt(0xC07F); __builtin_amdgcn_s_barrier(); asm volatile("" ::: "memory"); } while (0)
#define WAIT_BAR(N) asm volatile("s_waitcnt vmcnt(" #N ") lgkmcnt(0)\n\ts_barrier" ::: "memory")
DI void st_wt(char* p, u32x4 v) { asm volatile("global_store_dwordx4 %0, %1, off sc1\n\ts_nop 1" :: "v"(p), "v"(v) : "memory"); }
DI void st_wt8(char* p, u32x2 v) { asm volatile("global_store_dwordx2 %0, %1, off sc1" :: "v"(p), "v"(v) : "memory"); }
DI int swap23(int v) { return (v & ~12) | ((v & 4) << 1) | ((v & 8) >> 1); }
DI int vpos(int r) {
    const int lo = (int)((0x76543EDCBA983210ull >> (4 * (r & 15))) & 15) + (r == 11 ? 16 : 0);
    return r < 16 ? lo : (r < 19 ? r : 15);
}
constexpr unsigned ONES2 = 0x3f803f80u;
template <int SLEEP> DI void poll_ge(GAS unsigned* p, unsigned want) {
    for (unsigned spin = 0; !__all(__hip_atomic_load(p, __ATOMIC_RELAXED, __HIP_MEMORY_SCOPE_AGENT) >= want) && spin < (1u << 22) / SLEEP; ++spin) __builtin_amdgcn_s_sleep(SLEEP);
    asm volatile("" ::: "memory");
}
template <int SLEEP = 0, int LANES = 1> DI void poll_rmw(GAS unsigned* p, unsigned want, unsigned voff = 0) {
    for (unsigned spin = 0; spin < (1u << 21); ++spin) {
        unsigned v = ~0u, z;
        asm volatile("v_mov_b32 %1, 0\n\ts_mov_b64 exec, %4\n\tglobal_atomic_add %0, %2, %1, %3 sc0\n\ts_mov_b64 exec, -1\n\ts_waitcnt vmcnt(0)"
                     : "+v"(v), "=&v"(z) : "v"(voff), "s"(p), "i"((1 << LANES) - 1) : "memory");
        if (__all(v >= want)) break;
        if (SLEEP) __builtin_amdgcn_s_sleep(SLEEP);
    }
}
#define SYM_ADDR(dst, sym) do { int o_; asm volatile("s_getpc_b64 %0\n\ts_mov_b32 %1, " #sym "@rel32@lo+4" : "=s"(dst), "=s"(o_)); dst += (long long)o_; } while (0)
DI void touch(const void* p) { unsigned t; asm volatile("global_load_dword %0, %1, off\n\ts_waitcnt vmcnt(0)" : "=v"(t) : "v"(p) : "memory"); }

#define WLOAD(i) asm volatile("global_load_dword %0, %1, %2 offset:%3" : "=v"(wr[(i) >> 3][(i) & 7]) \
                              : "v"(wo), "s"((const char*)wqkv + (size_t)(((i) >> 3) * 32 + ((i) & 6)) * 3456), "i"(((i) & 1) * 3456) : "memory")
#define WWAIT(k, n) asm volatile("s_waitcnt vmcnt(%8)" : "+v"(wr[k][0]), "+v"(wr[k][1]), "+v"(wr[k][2]), "+v"(wr[k][3]), "+v"(wr[k][4]), "+v"(wr[k][5]), \
                                 "+v"(wr[k][6]), "+v"(wr[k][7]) : "i"(n))
#define WPERM(F, k, j) do { F[(j) >> 2][(j) & 3] = __builtin_amdgcn_perm(wr[k][2 * ((j) & 3) + 1], wr[k][2 * ((j) & 3)], (j) < 4 ? 0x05040100u : 0x07060302u); \
                            PIN(F[(j) >> 2][(j) & 3]); } while (0)
constexpr int K1UP = 24, K1PER = 7, K1PRE = 96;
template <int KIND> DI void k1_body(const u16* x, const u16* wqkv, char* ws, char* lds, int mb, int col0, int w, int l) {
    const int i16 = l & 15, q = l >> 4;
    u32x4 xr[9];
    {
        const char* xp = (const char*)x + (size_t)(mb * 64 + w * 8 + (l >> 3)) * (D_ * 2) + (l & 7) * 16;
#pragma unroll
        for (int n = 0; n < 9; ++n) asm volatile("global_load_dwordx4 %0, %1, off offset:%2" : "=v"(xr[n]) : "v"(xp), "i"(n * 128) : "memory");
    }
    const unsigned wo = (unsigned)(8 * q * 3456 + col0 * 2 + 4 * i16);
    unsigned wr[18][8];
    if (KIND < 2) {
#pragma unroll
        for (int i = 0; i < K1UP; ++i) WLOAD(i);
    }
    STAMP(0, 1);
    asm volatile("s_waitcnt vmcnt(%9)" : "+v"(xr[0]), "+v"(xr[1]), "+v"(xr[2]), "+v"(xr[3]), "+v"(xr[4]), "+v"(xr[5]), "+v"(xr[6]), "+v"(xr[7]), "+v"(xr[8])
                 : "i"(KIND < 2 ? K1UP : 0));
    {
        float s1 = 0.f, s2 = 0.f, t1 = 0.f, t2 = 0.f;
#pragma unroll
        for (int n = 0; n < 9; ++n)
#pragma unroll
            for (int e = 0; e < 4; e += 2) {
                s1 = dot2(xr[n][e], ONES2, s1); s2 = dot2(xr[n][e], xr[n][e], s2);
                t1 = dot2(xr[n][e + 1], ONES2, t1); t2 = dot2(xr[n][e + 1], xr[n][e + 1], t2);
            }
        SBAR();
        if (KIND < 2) {
#pragma unroll
            for (int i = K1UP; i < K1PRE - 9 * K1PER; ++i) WLOAD(i);
        }
        SBAR();
        s1 += t1; s2 += t2;
        s1 = dpp_add<DPP_X1>(s1); s2 = dpp_add<DPP_X1>(s2); s1 = dpp_add<DPP_X2>(s1); s2 = dpp_add<DPP_X2>(s2); s1 = dpp_add<DPP_HMIR>(s1); s2 = dpp_add<DPP_HMIR>(s2);
        const float mean = s1 * (1.f / D_), var = fmaxf(s2 * (1.f / D_) - mean * mean, 0.f);
        const float nm = -rbf(mean), rs = __builtin_amdgcn_rcpf(rbf(__builtin_amdgcn_sqrtf(rbf(rbf(var) + LN_EPS))));
        const f32x2 nm2 = {nm, nm}, rs2 = {rs, rs};
        if (KIND == 0 && col0 < 256 && (l & 7) == 0) *(GAS float*)(ws + WS_XM + (mb * 64 + w * 8 + (l >> 3)) * 4) = mean;
        const int t = w * 8 + (l >> 3);
        const int mt = (t >> 5) * 2 + ((t >> 2) & 1), row = ((t >> 3) & 3) * 4 + (t & 3);
        LAS char* dst = (LAS char*)lds + mt * 18432 + (l & 7) * 256 + ((row ^ (l & 7)) * 16);
#pragma unroll
        for (int n = 0; n < 9; ++n) {
            u32x4 y;
#pragma unroll
            for (int e = 0; e < 4; ++e) {
                const f32x2 c = f32x2{bflo(xr[n][e]), bfhi(xr[n][e])} + nm2;
                const unsigned d = pk2(c[0], c[1]);
                const f32x2 z = f32x2{bflo(d), bfhi(d)} * rs2;
                y[e] = pk2(z[0], z[1]);
            }
            *(LAS u32x4*)(dst + n * 2048) = y;
            SBAR();
            if (KIND < 2) {
#pragma unroll
                for (int i = K1PRE - (9 - n) * K1PER; i < K1PRE - (8 - n) * K1PER; ++i) WLOAD(i);
            }
            SBAR();
        }
    }
    char* kv0 = ws + WS_KV + (size_t)(mb * 2) * 2560;
    u32x4 f[2][2];
    if (KIND < 2) {
        WWAIT(0, K1PRE - 8 < 63 ? K1PRE - 8 : 63);
#pragma unroll
        for (int j = 0; j < 8; ++j) WPERM(f[0], 0, j);
    }
    asm volatile("s_waitcnt lgkmcnt(0)\n\ts_barrier" ::: "memory");
    STAMP(0, 4);
    if (KIND == 2) {
        if (mb < 12 && (w & 1)) *(GAS unsigned*)(ws + WS_CT + (mb * 64 + l) * 256) = 0u;
        const unsigned v = (w & 1) ? 0u : ONES2;
#pragma unroll
        for (int j = 0; j < 4; ++j)
            *(GAS u32x4*)(kv0 + (size_t)(j * 8 + (l >> 3)) * 163840 + ((l >> 2) & 1) * 2560 + 1280 + (l & 3) * 320 + ((w & 1) ? 15 : 18) * 16) = u32x4{v, v, v, v};
        return;
    }
    const LAS char* xa = (const LAS char*)lds + q * 256 + ((i16 ^ q) * 16);
    const LAS char* xb = (const LAS char*)lds + q * 256 + ((i16 ^ (4 + q)) * 16);
    f32x4 acc[2][4];
    u32x4 xf[2][4];
#pragma unroll
    for (int m = 0; m < 4; ++m) { acc[0][m] = acc[1][m] = f32x4{0, 0, 0, 0}; xf[0][m] = *(const LAS u32x4*)(xa + m * 18432); }
    SBAR();
#pragma unroll
    for (int k = 0; k < 18; ++k) {
        constexpr int NL = (144 - K1PRE) / 8;
        if (k < 17) { const int yg = K1PRE + 8 * (k < NL ? k : NL) - 8 * (k + 2); WWAIT(k + 1, yg < 63 ? yg : 63); }
#pragma unroll
        for (int j = 0; j < 8; ++j) {
            const int m = j >> 1, t = j & 1;
            acc[t][m] = KIND ? mfma16(xf[k & 1][m], f[k & 1][t], acc[t][m]) : mfma16(f[k & 1][t], xf[k & 1][m], acc[t][m]);
            SBAR();
            if (k < 17) {
                if (j < 4) xf[(k + 1) & 1][j] = *(const LAS u32x4*)(((k & 1) ? xa : xb) + j * 18432 + (k + 1) * 1024);
                if (j < 7) WPERM(f[(k + 1) & 1], k + 1, j);
                if (j == 6) WPERM(f[(k + 1) & 1], k + 1, 7);
            }
            if (k < NL) WLOAD(8 * k + K1PRE + j);
            SBAR();
        }
    }
    STAMP(0, 2);
    if (KIND == 1) {
#pragma unroll
        for (int t = 0; t < 2; ++t) {
            const int n = col0 - 1152 + 2 * i16 + t, hd = n / 18, d = n - 18 * hd, pos = vpos(d < 16 ? swap23(d) : d);
#pragma unroll
            for (int tl = 0; tl < 2; ++tl)
                *(GAS u32x4*)(kv0 + (size_t)hd * 163840 + tl * 2560 + 1280 + q * 320 + pos * 16) = u32x4{pk2(acc[t][2 * tl][0], acc[t][2 * tl][1]), pk2(acc[t][2 * tl][2], acc[t][2 * tl][3]),
                          pk2(acc[t][2 * tl + 1][0], acc[t][2 * tl + 1][1]), pk2(acc[t][2 * tl + 1][2], acc[t][2 * tl + 1][3])};
        }
    } else {
        const bool isq = col0 < 576;
        const int cg = (isq ? col0 : col0 - 576) / 8 + q, grp = cg / 9, c = cg - 9 * grp, cm = c > 4 ? c - 1 : c;
        u32x4 o[4];
#pragma unroll
        for (int m = 0; m < 4; ++m)
#pragma unroll
            for (int r = 0; r < 4; ++r) o[m][r] = pk2(acc[0][m][r], acc[1][m][r]);
        const int t5 = 8 * (i16 >> 2) + (i16 & 3), rho = swap23(t5);
        if (isq) {
            char* dst = ws + WS_Q + ((size_t)cg * 2048 + mb * 64 + t5) * 16;
#pragma unroll
            for (int m = 0; m < 4; ++m) {
#pragma unroll
                for (int r = 0; r < 4; ++r) o[m][r] = pk2(bflo(o[m][r]) * QSCALE, bfhi(o[m][r]) * QSCALE);
                st_wt((dst + (m >> 1) * 512 + (m & 1) * 64), o[m]);
            }
        } else {
            char* kt = kv0 + (size_t)(4 * grp) * 163840;
            if (c != 4) {
                char* dst = kt + (size_t)(cm >> 1) * 163840 + (cm & 1) * 512 + rho * 16;
#pragma unroll
                for (int m = 0; m < 4; ++m) st_wt((dst + (m >> 1) * 2560 + (m & 1) * 128), o[m]);
            }
            char* d2 = kt + (size_t)(c / 2) * 163840 + 1024 + rho * 8;
            if (c == 2 || c == 4) {
#pragma unroll
                for (int m = 0; m < 4; ++m) st_wt8((d2 - 163840 + (m >> 1) * 2560 + (m & 1) * 64), u32x2{o[m][0], o[m][1]});
            }
            if (c == 4 || c == 6) {
#pragma unroll
                for (int m = 0; m < 4; ++m) st_wt8((d2 + (m >> 1) * 2560 + (m & 1) * 64), u32x2{o[m][2], o[m][3]});
            }
        }
    }
    STAMP(0, 3);
}

extern "C" __global__ __launch_bounds__(512, 2) void k1_qkv(const u16* __restrict__ x, const u16* __restrict__ wqkv, char* __restrict__ ws) {
    __shared__ __attribute__((aligned(16))) char lds[73728];
    const int b = blockIdx.x, mb = 4 * (b & 7) + ((b >> 3) & 3), jb = ((b >> 5) + 7) & 7;
    const int tid = threadIdx.x, l = tid & 63, w = __builtin_amdgcn_readfirstlane(tid >> 6);
    STAMP(0, 0);
    if (jb == 7) {
        unsigned long long c0, c1;
        SYM_ADDR(c0, k1_qkv); SYM_ADDR(c1, k9_end);
        const unsigned long long o = (unsigned long long)((((b >> 3) & 3) * 8 + w) * 64 + l) * 64;
        unsigned t0;
        const unsigned wl = ((b * 8 + w) * 64 + l) * 128;
        asm volatile("global_load_dword %0, %1, %2" : "=v"(t0) : "v"(wl < 1990656u ? wl : 0u), "s"(wqkv) : "memory");
        if (c0 + o < c1) touch((const void*)(c0 + o));
        unsigned t1;
        const unsigned pg = ((((b >> 3) & 3) * 8 + w) * 64 + l) * 32768;
        asm volatile("global_load_dword %0, %1, %2" : "=v"(t1) : "v"(pg < (unsigned)WS_CT ? pg : 0u), "s"(ws) : "memory");
        asm volatile("s_waitcnt vmcnt(0)" : "+v"(t0), "+v"(t1));
        return;
    }
    const int col0 = 256 * jb + 32 * w;
    if (col0 < 1152) k1_body<0>(x, wqkv, ws, lds, mb, col0, w, l);
    else if (col0 < 1728) k1_body<1>(x, wqkv, ws, lds, mb, col0, w, l);
    else k1_body<2>(x, wqkv, ws, lds, mb, col0, w, l);
}

DI void wd_rows(int kap, int& A, int& B) {
    const int W = kap / 3, qq = kap - 3 * W, T0 = 3 * W + (qq == 2 ? 2 : 0), T1 = 3 * W + (qq == 2 ? 2 : 1);
    A = 64 * (T0 >> 3) + (T0 & 7) + (qq == 2 ? 0 : 16 * qq);
    B = 64 * (T1 >> 3) + (T1 & 7) + (qq == 2 ? 16 : 16 * qq);
}

DI void wo_rows(int kap, int& A, int& B) {
    A = kap < 64 ? 18 * (kap >> 1) + 8 * (kap & 1) : 72 * (kap - 64) + 16;
    B = A + (kap < 64 ? 4 : 36);
}
#define ST_WT_S(sb, vo, off, v) asm volatile("global_store_dwordx4 %0, %1, %2 offset:%3 sc1\n\ts_nop 1" :: "v"(vo), "v"(v), "s"(sb), "i"(off) : "memory")
DI void retile(int xcd, int v, int l, const u16* wo, const u16* wg, const u16* wu, const u16* wd, char* ws) {
    const int kq = l >> 4, i16 = l & 15, i8 = l & 7, par = (l >> 3) & 1;
    u32x4 sd[8];
    auto col = [&](int t) {
        u32x4 f;
#pragma unroll
        for (int a = 0; a < 4; ++a) f[a] = __builtin_amdgcn_perm(sd[2 * a + 1][t >> 1], sd[2 * a][t >> 1], (t & 1) ? 0x07060302u : 0x05040100u);
        return f;
    };
    if (v < 162) {
        const int u = 9 * xcd + v / 18, ks = v % 18;
        const unsigned vo = (8 * kq * F_ + 8 * i8) * 2;
        const size_t so = (size_t)(32 * ks * F_ + 64 * u) * 2;
#pragma unroll
        for (int j = 0; j < 8; ++j)
            asm volatile("s_mov_b64 exec, %4\n\tglobal_load_dwordx4 %0, %1, %2\n\ts_not_b64 exec, exec\n\tglobal_load_dwordx4 %0, %1, %3\n\ts_mov_b64 exec, -1"
                         : "=&v"(sd[j]) : "v"(vo), "s"((const char*)wg + so + j * F_ * 2), "s"((const char*)wu + so + j * F_ * 2), "s"(0x00ff00ff00ff00ffull) : "scc", "memory");
        asm volatile("s_waitcnt vmcnt(0)" : "+v"(sd[0]), "+v"(sd[1]), "+v"(sd[2]), "+v"(sd[3]), "+v"(sd[4]), "+v"(sd[5]), "+v"(sd[6]), "+v"(sd[7]));
        const unsigned vd = kq * 256 + (4 * (i8 >> 1) + 2 * par + (i8 & 1)) * 16;
        char* dst = ws + WS_GU + ((size_t)(8 * u) * 18 + ks) * 1024;
#pragma unroll
        for (int t = 0; t < 8; ++t) ST_WT_S(dst + t * 18432, vd, 0, col(t));
    } else if (v < 243) {
        const bool isd = v < 234;
        const int ks = 18 * xcd + ((v - 162) >> 2), nb = (v - 162) & 3, pr = v - 234;
        int A0, B0, A1, B1;
        if (isd) { wd_rows(4 * ks + kq, A0, B0); A1 = A0; B1 = B0; } else { wo_rows(8 * pr + kq, A0, B0); wo_rows(8 * pr + 4 + kq, A1, B1); }
        const unsigned c0 = 8 * i16, c1 = c0 + (isd ? 128 : 0), cl = isd ? 256 + 2 * i16 : 128 + 2 * i8;
        const unsigned o0[2] = {A0 * (D_ * 2) + c0, B0 * (D_ * 2) + c0}, o1[2] = {A1 * (D_ * 2) + c1, B1 * (D_ * 2) + c1};
        const unsigned ol[2] = {(par ? A1 : A0) * (D_ * 2) + cl, (par ? B1 : B0) * (D_ * 2) + cl};
        const GAS char* src = isd ? (const GAS char*)wd + 288 * nb : (const GAS char*)wo + 144 * xcd;
        const int s0 = (isd ? 8 : 1) * D_ * 2, s1 = (isd ? 32 : (pr < 8 ? 2 : 18)) * D_ * 2;
        unsigned sl[8];
#pragma unroll
        for (int j = 0; j < 8; ++j) {
            const GAS char* sb = src + ((j >> 1) & 1) * s1 + (j & 1) * s0;
            const u32x2 a = *(const GAS u32x2*)(sb + o0[j >> 2]), c = *(const GAS u32x2*)(sb + o1[j >> 2]);
            sd[j] = u32x4{a[0], a[1], c[0], c[1]};
            sl[j] = *(const GAS unsigned short*)(sb + ol[j >> 2]);
        }
        char* dst = isd ? ws + WS_WD + ((size_t)nb * 144 + ks) * 9216 : ws + WS_WO + ((size_t)xcd * 9 + pr) * 9216;
#pragma unroll
        for (int t = 0; t < 8; ++t) ST_WT_S(dst + (t >> 2) * 4096, l * 16, (t & 3) * 1024, col(t));
        ST_WT_S(dst + 8192, l * 16, 0, (u32x4{sl[0] | (sl[1] << 16), sl[2] | (sl[3] << 16), sl[4] | (sl[5] << 16), sl[6] | (sl[7] << 16)}));
    } else if (v < 252) {
        const int kap = 9 * xcd + v - 243, e = l >> 3;
        int A, B;
        wo_rows(kap, A, B);
        const GAS char* src = (const GAS char*)wo + ((e < 4 ? A : B) + ((e >> 1) & 1) * (kap < 64 ? 2 : 18) + (e & 1)) * (D_ * 2) + i8 * 16;
        float sum = 0.f;
#pragma unroll
        for (int n = 0; n < 9; ++n) {
            const u32x4 x = *(const GAS u32x4*)(src + n * 128);
#pragma unroll
            for (int j = 0; j < 4; ++j) sum = dot2(x[j], ONES2, sum);
        }
        sum = dpp_add<DPP_X1>(sum); sum = dpp_add<DPP_X2>(sum); sum = dpp_add<DPP_HMIR>(sum);
        sum *= 1.f / D_;
        const float hi = rbf(sum);
        if (i8 < 2) __hip_atomic_store((GAS unsigned short*)(ws + WS_WB + kap * 32 + i8 * 16 + e * 2), (unsigned short)pk2(i8 ? sum - hi : hi, 0.f), __ATOMIC_RELAXED, __HIP_MEMORY_SCOPE_AGENT);
    }
    if (v >= 252) return;
    asm volatile("s_waitcnt vmcnt(0)" ::: "memory");
    const bool isd = v >= 162 && v < 234;
    GAS unsigned* ct = (GAS unsigned*)(ws + WS_CT) + (isd ? 96 * 64 : 0);
    unsigned n = 0;
    if (l == 0 && __hip_atomic_fetch_add(ct + (545 + xcd) * 64, 1u, __ATOMIC_RELAXED, __HIP_MEMORY_SCOPE_AGENT) == (isd ? 71u : 179u))
        n = __hip_atomic_fetch_add(ct + 544 * 64, 1u, __ATOMIC_RELAXED, __HIP_MEMORY_SCOPE_AGENT);
    if (__builtin_amdgcn_readfirstlane(n) == 7u) __hip_atomic_store(ct + (576 + l) * 64, 1u, __ATOMIC_RELAXED, __HIP_MEMORY_SCOPE_AGENT);
}

constexpr int L_ST = 147456, L_FL = L_ST + 4096;
DI void k2_helper(const u16* wo, const u16* wg, const u16* wu, const u16* wd, char* ws, char* lds, int w) {
    const int b = blockIdx.x, xcd = b & 7, bi = b >> 3, head = 4 * xcd + (bi & 3);
    const int l = threadIdx.x & 63, hw = w - 4;
    const unsigned lds0 = (unsigned)(uintptr_t)lds;
    const char* kvsrc = ws + WS_KV + (size_t)head * 163840;
    if (hw >= 4) asm volatile("s_barrier\n\ts_barrier" ::: "memory");
    else {
        gldsn<4>(kvsrc + hw * 4096, l * 16, lds0 + hw * 4096);
        gldsn<4>(kvsrc + (16 + 4 * hw) * 1024, l * 16, lds0 + (16 + 4 * hw) * 1024);
        WAIT_BAR(4);
#pragma unroll
        for (int j = 0; j < 8; ++j) gldsn<4>(kvsrc + (32 + j * 16 + 4 * hw) * 1024, l * 16, lds0 + (32 + j * 16 + 4 * hw) * 1024);
        WAIT_BAR(0);
    }
    __builtin_amdgcn_s_setprio(3);
    const int u = hw * 32 + bi;
    if (u >= 180) __builtin_amdgcn_s_sleep(DSL);
    retile(xcd, u < 18 ? 234 + u : (u < 252 ? u - 18 : u), l, wo, wg, wu, wd, ws);
    __builtin_amdgcn_s_setprio(0);
    if (hw < 4) {
        unsigned long long c0, c1;
        SYM_ADDR(c0, k2_main); SYM_ADDR(c1, k9_end);
        const unsigned long long o = (unsigned long long)((bi * 4 + hw) * 64 + l) * 64;
        if (c0 + o < c1) touch((const void*)(c0 + o));
    }
    poll_ge<16>((GAS unsigned*)(ws + WS_CT) + (576 + (b & 63)) * 64, 1u);
}
DI void k2_helper_end(char* ws, char* lds, int w) {
    const int b = blockIdx.x, nb = 4 * ((b >> 1) & 1) + ((b >> 3) & 3), l = threadIdx.x & 63, hw = w - 4;
    if (hw >= 4) {
        {
            const unsigned ln = (b >> 5) * 216 + (hw - 4) * 54 + l;
            unsigned t[3];
#pragma unroll
            for (int j = 0; j < PFN; ++j)
                asm volatile("global_load_dword %0, %1, off sc1" : "=v"(t[j]) : "v"(ws + WS_GU + (size_t)((4 * ((b & 7) >> 1) + ((b >> 3) & 3)) * 12 + 4 * j) * 55296 + (ln < 1728 ? ln * 128 : 0)) : "memory");
#pragma unroll
            for (int j = 0; j < PFN; ++j) asm volatile("s_waitcnt vmcnt(0)" : "+v"(t[j]));
        }
        STAMP(1, 6);
        asm volatile("s_barrier" ::: "memory");
        *(volatile LAS unsigned*)(lds + L_FL + l * 4) = 0;
        LBAR();
        return;
    }
    u32x4 wv[21];
#pragma unroll
    for (int t = 0; t < 21; ++t) {
        const int o = (hw * 21 + t) * 1024;
        asm volatile("global_load_dwordx4 %0, %1, %2 sc1" : "=v"(wv[t]) : "v"(l * 16), "s"(o < 82944 ? ws + WS_WO + (size_t)nb * 82944 + o : ws + WS_WB + (o - 82944)) : "memory");
    }
    STAMP(1, 6);
    asm volatile("s_waitcnt vmcnt(0)\n\ts_barrier" ::: "memory");
#pragma unroll
    for (int t = 0; t < 21; ++t) { PIN(wv[t]); *(LAS u32x4*)((LAS char*)lds + (hw * 21 + t) * 1024 + l * 16) = wv[t]; }
    LBAR();
}

DI void k2_attn(const u16* x, char* ws, char* lds, int w) {
    const int b = blockIdx.x, xcd = b & 7, bi = b >> 3, hh = bi & 3, head = 4 * xcd + hh, qb = bi >> 2;
    const int l = threadIdx.x & 63;
    const int mb = 16 * (b & 1) + 2 * (b >> 5) + (xcd >> 2), nb = 4 * ((b >> 1) & 1) + hh;
    GAS unsigned* ct = (GAS unsigned*)(ws + WS_CT) + 512 * 64;
    const int r32 = l & 31, h = l >> 5;
    const int qrow = qb * 256 + w * 64 + r32;
    const char* qp = ws + WS_Q + ((size_t)xcd * 9 * 2048 + qrow) * 16;
    u32x4 q1[2]; u32x2 q2[2];
#pragma unroll
    for (int i = 0; i < 2; ++i) {
        q1[i] = *(const GAS u32x4*)(qp + (size_t)(2 * hh + (hh >> 1) + h) * 32768 + i * 512);
        q2[i] = *(const GAS u32x2*)(qp + (size_t)(2 + 2 * ((hh + 1) >> 1)) * 32768 + (hh >> 1) * 8 + i * 512);
    }
    const LAS char* const kp0 = (const LAS char*)lds + h * 512 + r32 * 16;
    const LAS char* const k2p0 = (const LAS char*)lds + (h ? 1280 + 18 * 16 : 1024 + r32 * 8);
    const LAS char* const vp0 = (const LAS char*)lds + 1280 + h * 320 + vpos(r32) * 16;
#pragma unroll
    for (int i = 0; i < 2; ++i) {
        if (hh == 1 && h == 0) q1[i][0] = 0;
        if (hh == 2 && h == 1) q1[i][3] = 0;
        if (hh == 3 || h) q2[i][0] = 0;
        if (hh == 0 || h) q2[i][1] = 0;
        PIN(q1[i]); PIN(q2[i]);
    }
    const int i16 = l & 15, q = l >> 4, row = mb * 64 + w * 16 + i16;
    u32x4 r0, r1; u32x2 r2; float xm;
    {
        const char* xp = (const char*)x + ((size_t)row * D_ + 72 * nb) * 2;
        asm volatile("global_load_dwordx4 %0, %4, off\n\tglobal_load_dwordx4 %1, %4, off offset:16\n\tglobal_load_dwordx2 %2, %5, off\n\tglobal_load_dword %3, %6, off"
                     : "=&v"(r0), "=&v"(r1), "=&v"(r2), "=&v"(xm) : "v"(xp + q * 32), "v"(xp + 128 + (q & 1) * 8), "v"(ws + WS_XM + row * 4) : "memory");
    }
    WAIT_BAR(4);
    __builtin_amdgcn_s_setprio(2);
    STAMP(1, 1);
    f32x16 o[2];
    float lsum[2];
    for (int attempt = 0;; ++attempt) {
        float mx[2] = {-3.0e38f, -3.0e38f};
        const int nt = attempt ? 64 : 1;
        if (h) { q2[0] = u32x2{0, 0}; q2[1] = u32x2{0, 0}; }
        u32x4 kf[2], v0[2], v1[2]; u32x2 k2[2];
        f32x16 sa, sb;
        v0[0] = *(const LAS u32x4*)vp0; v1[0] = *(const LAS u32x4*)(vp0 + 640);
        for (int t = nt - 1; t >= 0; --t) {
            k2[0] = *(const LAS u32x2*)(k2p0 + t * 2560); kf[0] = *(const LAS u32x4*)(kp0 + t * 2560);
            sa = mfma32k8(k2[0], q2[0], f32x16{}); sb = mfma32k8(k2[0], q2[1], f32x16{});
            sa = mfma32(kf[0], q1[0], sa); sb = mfma32(kf[0], q1[1], sb);
#pragma unroll
            for (int r = 0; r < 16; ++r) { mx[0] = fmaxf(mx[0], sa[r]); mx[1] = fmaxf(mx[1], sb[r]); }
        }
#pragma unroll
        for (int i = 0; i < 2; ++i) {
            const auto sw = __builtin_amdgcn_permlane32_swap(__float_as_uint(mx[i]), __float_as_uint(mx[i]), false, false);
            mx[i] = fmaxf(__uint_as_float(sw[0]), __uint_as_float(sw[1]));
            const float hi = rbf(-mx[i]), r1 = -mx[i] - hi, mid = rbf(r1);
            if (h) q2[i] = u32x2{pk2(hi, mid), pk2(r1 - mid, 0.f)};
            o[i] = f32x16{};
        }
        sa -= mx[0];
        const LAS char* kp = kp0; const LAS char* k2p = k2p0; const LAS char* vp = vp0;
        v1[1] = u32x4{0, 0, 0, 0};
        u32x4 p0, p1 = {0, 0, 0, 0};
        float e0 = 0.f, e1 = 0.f, e2 = 0.f, e3 = 0.f;
        __builtin_amdgcn_s_waitcnt(0xC07F);
#define SLOT(MF, LD, P, i, C, j) do { MF; SBAR(); LD; P[i] = pk2(e0, e1); P[i + 1] = pk2(e2, e3); PIN(P[i]); PIN(P[i + 1]); SBAR(); \
            e0 = __builtin_amdgcn_exp2f(C[j]); e1 = __builtin_amdgcn_exp2f(C[j + 1]); e2 = __builtin_amdgcn_exp2f(C[j + 2]); e3 = __builtin_amdgcn_exp2f(C[j + 3]); \
            PIN(e0); PIN(e1); PIN(e2); PIN(e3); SBAR(); } while (0)
#define STEP(c, KN, K2N, VN) do { \
            SLOT(sb = mfma32k8(k2[c], q2[1], f32x16{}), k2[c ^ 1] = *(const LAS u32x2*)(K2N); kf[c ^ 1] = *(const LAS u32x4*)(KN), p1, 2, sa, 0); \
            SLOT(o[1] = mfma32(v1[c ^ 1], p1, o[1]), v0[c ^ 1] = *(const LAS u32x4*)(VN); v1[c ^ 1] = *(const LAS u32x4*)((VN) + 640), p0, 0, sa, 4); \
            SLOT(sb = mfma32(kf[c], q1[1], sb), , p0, 2, sa, 8); \
            SLOT(o[0] = mfma32(v0[c], p0, o[0]), , p1, 0, sa, 12); \
            __builtin_amdgcn_s_waitcnt(0xC07F); SBAR(); \
            SLOT(sa = mfma32k8(k2[c ^ 1], q2[0], f32x16{}), , p1, 2, sb, 0); \
            SLOT(o[0] = mfma32(v1[c], p1, o[0]), , p0, 0, sb, 4); \
            SLOT(sa = mfma32(kf[c ^ 1], q1[0], sa), , p0, 2, sb, 8); \
            SLOT(o[1] = mfma32(v0[c], p0, o[1]), , p1, 0, sb, 12); } while (0)
        constexpr int UN = 8;
        for (int it = 0; it < 64 / UN; ++it) {
            const int dl = it == 64 / UN - 1 ? (UN - 64) * 2560 : UN * 2560;
            const LAS char* kn = kp + dl; const LAS char* k2n = k2p + dl; const LAS char* vn = vp + dl;
#pragma unroll
            for (int j = 1; j < UN; j += 2) {
                STEP(0, kp + j * 2560, k2p + j * 2560, vp + j * 2560);
                if (j == 5) asm volatile("s_cmp_lg_u32 %0, 0\n\ts_cbranch_scc1 .Lnb%=\n\ts_barrier\n.Lnb%=:" :: "s"(attempt + it) : "scc", "memory");
                STEP(1, j + 1 < UN ? kp + (j + 1) * 2560 : kn, j + 1 < UN ? k2p + (j + 1) * 2560 : k2n, j + 1 < UN ? vp + (j + 1) * 2560 : vn);
            }
            kp = kn; k2p = k2n; vp = vn;
        }
        p1[2] = pk2(e0, e1); p1[3] = pk2(e2, e3);
        o[1] = mfma32(v1[1], p1, o[1]);
#undef STEP
#undef SLOT
        bool ok = true;
#pragma unroll
        for (int i = 0; i < 2; ++i) {
            lsum[i] = __uint_as_float(__builtin_amdgcn_permlane32_swap(__float_as_uint(o[i][10]), __float_as_uint(o[i][10]), false, false)[0]);
            ok &= lsum[i] < 1.0e30f;
#pragma unroll
            for (int r = 0; r < 10; ++r) ok &= fabsf(o[i][r]) < 3.0e38f;
        }
        if (attempt || __all(ok)) break;
    }
    STAMP(1, 2);
#pragma unroll
    for (int i = 0; i < 2; ++i) {
        const float inv = __builtin_amdgcn_rcpf(lsum[i]);
        char* of = ws + WS_OF + (size_t)((qrow >> 4) + 2 * i) * 72 * 256 + (qrow & 15) * 16;
        st_wt(of + (2 * head + h) * 256, u32x4{pk2(o[i][0] * inv, o[i][1] * inv), pk2(o[i][2] * inv, o[i][3] * inv), pk2(o[i][4] * inv, o[i][5] * inv), pk2(o[i][6] * inv, o[i][7] * inv)});
        if (h == 0) __hip_atomic_store((GAS unsigned*)(of + (64 + (head >> 2)) * 256 + (head & 3) * 4), pk2(o[i][8] * inv, o[i][9] * inv), __ATOMIC_RELAXED, __HIP_MEMORY_SCOPE_AGENT);
    }
    STAMP(1, 3);
    asm volatile("s_waitcnt lgkmcnt(0)\n\ts_barrier" ::: "memory");
    const unsigned lo = l * 16;
    asm volatile("s_waitcnt vmcnt(0)" : "+v"(r0), "+v"(r1), "+v"(r2), "+v"(xm) :: "memory");
    if (l == 0) __hip_atomic_fetch_add(ct + (4 * qb + w) * 64, 1u, __ATOMIC_RELAXED, __HIP_MEMORY_SCOPE_AGENT);
    auto hoff = [&](int c) { const int kap = 9 * nb + c; return (unsigned)((kap >> 2) * 8192 + (kap & 3) * 256 + i16 * 16); };
    unsigned h0 = hoff(2 * q), h1 = hoff(2 * q + 1), h2 = hoff(8) + q * 8;
    PIN(h0); PIN(h1); PIN(h2);
    f32x4 acc[7];
#pragma unroll
    for (int t = 0; t < 7; ++t) acc[t] = f32x4{0, 0, 0, 0};
    const LAS char* wp = (const LAS char*)lds + lo;
    const LAS char* mp = (const LAS char*)lds + 82944 + q * 32 + (i16 & 1) * 16;
    PIN(mp);
    u32x4 wf[11];
    auto wfrag = [&](int pr, int j) { return *(const LAS u32x4*)(j < 9 ? wp + pr * 9216 + j * 1024 : mp + (2 * pr + j - 9) * 128); };
    LBAR();
#pragma unroll
    for (int j = 0; j < 11; ++j) { wf[j] = wfrag(0, j); PIN(wf[j]); }
    {
        volatile LAS unsigned* rdy = (volatile LAS unsigned*)(lds + L_FL + 36);
        if (w == 0) { poll_rmw(ct + mb * 64, 32u); *rdy = 1u; }
        else while (__any(*rdy == 0u)) __builtin_amdgcn_s_sleep(1);
        asm volatile("" ::: "memory");
    }
    STAMP(2, 4);
    u32x4 of[18];
    {
        const auto rsrc = __builtin_amdgcn_make_buffer_rsrc(ws + WS_OF + (size_t)(mb * 4 + w) * 18432, 0, 18432, 0x00020000);
#pragma unroll
        for (int k = 0; k < 18; ++k) { of[k] = __builtin_amdgcn_raw_buffer_load_b128(rsrc, lo, k * 1024, 16); SBAR(); }
    }
    STAMP(2, 1);
#pragma unroll
    for (int pr = 0; pr < 9; ++pr)
#pragma unroll
        for (int i = 0; i < 12; ++i) {
            const int c = i / 6, t = i % 6, j = t < 4 ? c * 4 + t : (t == 4 ? 8 : 9 + c);
            f32x4& d = acc[t < 4 ? t : (t == 4 ? 4 + c : 6)];
            SBAR();
            d = mfma16(wf[j], of[2 * pr + c], d);
            SBAR();
            if (pr < 8 && i != 4) wf[j] = wfrag(pr + 1, j);
        }
    STAMP(2, 2);
    const float mean = xm + acc[6][0] + acc[6][1], nm = -rbf(mean);
    u32x4 o0, o1; u32x2 o2;
#pragma unroll
    for (int j = 0; j < 4; ++j) {
        o0[j] = pk2(acc[2 * (j & 1)][j >> 1] + bflo(r0[j]), acc[2 * (j & 1) + 1][j >> 1] + bfhi(r0[j]));
        o1[j] = pk2(acc[2 * (j & 1)][2 + (j >> 1)] + bflo(r1[j]), acc[2 * (j & 1) + 1][2 + (j >> 1)] + bfhi(r1[j]));
    }
    {
        float e[4];
#pragma unroll
        for (int r = 0; r < 4; ++r) {
            const auto sw = __builtin_amdgcn_permlane32_swap(__float_as_uint(acc[5][r]), __float_as_uint(acc[4][r]), false, false);
            e[r] = __uint_as_float(sw[1]) + acc[4][r];
        }
        o2 = u32x2{pk2(e[0] + bflo(r2[0]), e[1] + bfhi(r2[0])), pk2(e[2] + bflo(r2[1]), e[3] + bfhi(r2[1]))};
    }
    char* hb = ws + WS_HF + (size_t)(mb >> 1) * 147456 + ((mb & 1) * 4 + w) * 1024;
    char* hc = hb + (WS_HC - WS_HF);
    f32x2 ss = {0.f, 0.f}, s8 = {0.f, 0.f};
    u32x4 c0, c1; u32x2 c2;
    auto centre = [&](unsigned v, f32x2& acc2) {
        const f32x2 d = f32x2{bflo(v), bfhi(v)} + f32x2{nm, nm};
        acc2 = __builtin_elementwise_fma(d, d, acc2);
        return pk2(d[0], d[1]);
    };
#pragma unroll
    for (int j = 0; j < 4; ++j) { c0[j] = centre(o0[j], ss); c1[j] = centre(o1[j], ss); }
    st_wt((hc + h0), c0); st_wt((hc + h1), c1);
    c2[0] = centre(o2[0], s8); c2[1] = centre(o2[1], s8);
    if (q < 2) st_wt8((hc + h2), c2);
    float sq = (ss[0] + ss[1]) + (q < 2 ? s8[0] + s8[1] : 0.f) - 18.f * (mean + nm) * (mean + nm);
    { const auto a = __builtin_amdgcn_permlane16_swap(__float_as_uint(sq), __float_as_uint(sq), false, false); sq = __uint_as_float(a[0]) + __uint_as_float(a[1]); }
    { const auto a = __builtin_amdgcn_permlane32_swap(__float_as_uint(sq), __float_as_uint(sq), false, false); sq = __uint_as_float(a[0]) + __uint_as_float(a[1]); }
    if (q == 0) __hip_atomic_store((GAS float*)(ws + WS_ST + (size_t)row * 32 + nb * 4), sq, __ATOMIC_RELAXED, __HIP_MEMORY_SCOPE_AGENT);
    st_wt((hb + h0), o0); st_wt((hb + h1), o1);
    if (q < 2) st_wt8((hb + h2), o2);
    STAMP(2, 3);
}

template <bool CONS> DI void k4_body(char* ws, u16* out, char* lds, int w) {
    const bool LAST = !CONS && w >= 8;
    constexpr float K0 = -2.0f * 0.7978845608028654f * 1.4426950408889634f, K1 = K0 * 0.044715f;
    const int b = blockIdx.x, s = 4 * ((b & 7) >> 1) + ((b >> 3) & 3), mtile = 8 * (b & 1) + (b >> 5);
    const int W = 12 * s + w;
    const unsigned lds0 = (unsigned)(uintptr_t)lds;
    STAMP(3, 0);
    STAMP(4, 0);
    unsigned lo = (threadIdx.x & 63) * 16;
    const char* hsrc = ws + WS_HC + (size_t)mtile * 147456;
    auto hp3 = [&](int idx) { gldsn_dev<3>(hsrc + idx * 1024, lo, lds0 + idx * 1024); };
    const char* wp = ws + WS_GU + (size_t)W * 55296;
    u32x4 wf[4][3];
#define K4W(C, k, t) asm volatile("global_load_dwordx4 %0, %1, %2 offset:%3 sc1" : C(wf[(k) & 3][t]) \
                                  : "v"(lo), "s"(wp + (t) * 18432 + ((k) >> 2) * 4096), "i"(((k) & 3) * 1024) : "memory")
#define K4W3(C, k) do { K4W(C, k, 0); K4W(C, k, 1); K4W(C, k, 2); } while (0)
    LAS unsigned* flag = (LAS unsigned*)(lds + L_FL);
    const LAS char* hp = (const LAS char*)lds + lo;
    auto publish = [&](int ms) { if (lo == 0) __hip_atomic_fetch_add(flag, 1u << (4 * ms), __ATOMIC_RELAXED, __HIP_MEMORY_SCOPE_WORKGROUP); };
    GAS unsigned* hct = (GAS unsigned*)(ws + WS_CT) + (560 + mtile) * 64;
    if (w < 4) {
        asm volatile("s_waitcnt vmcnt(0)" ::: "memory");
        if (lo == 0 && __hip_atomic_fetch_add(flag + 6, 1u, __ATOMIC_RELAXED, __HIP_MEMORY_SCOPE_WORKGROUP) == 3u) __hip_atomic_fetch_add(hct, 1u, __ATOMIC_RELAXED, __HIP_MEMORY_SCOPE_AGENT);
    }
    K4W3("=v", 0); K4W3("=v", 1); K4W3("=v", 2); K4W3("=v", 3);
    if (w >= 4) k2_helper_end(ws, lds, w);
    f32x4 acc[3][8];
#pragma unroll
    for (int m = 0; m < 8; ++m)
#pragma unroll
        for (int t = 0; t < 3; ++t) { acc[t][m] = f32x4{0, 0, 0, 0}; PIN(acc[t][m]); }
    STAMP(3, 5);
    if (LAST) {
        __builtin_amdgcn_s_setprio(3);
        const int c = w - 8;
        if (c == 0) {
            while (__any(*(volatile LAS unsigned*)(flag + 6) < 4u)) __builtin_amdgcn_s_sleep(1);
            poll_rmw(hct, 16u);
            hp3(0); hp3(3); hp3(6); hp3(9);
            *(volatile LAS unsigned*)(flag + 7) = 1u;
        } else while (__any(*(volatile LAS unsigned*)(flag + 7) == 0u)) __builtin_amdgcn_s_sleep(1);
        STAMP(3, 6);
        asm volatile("" ::: "memory");
        hp3(12 + 3 * c); hp3(24 + 3 * c);
        WAIT_BAR(6);
#pragma unroll 1
        for (int g = 0; g < 4; ++g) {
            asm volatile("s_waitcnt vmcnt(0)" ::: "memory");
            publish(g);
            hp3(36 + 24 * g + 3 * c);
            asm volatile("s_waitcnt vmcnt(0)" ::: "memory");
            hp3(48 + 24 * g + 3 * c);
        }
        hp3(132 + 3 * c);
        gldsn_dev<1>(ws + WS_ST + (size_t)mtile * 4096 + c * 1024, lo, lds0 + L_ST + c * 1024);
        asm volatile("s_waitcnt vmcnt(4)" ::: "memory");
        publish(4);
        asm volatile("s_waitcnt vmcnt(0)" ::: "memory");
        LAS f32x4* sp = (LAS f32x4*)(hp + L_ST + c * 1024);
        const f32x4 sv = *sp;
        const float v2 = fmaxf(dpp_add<DPP_X1>((sv[0] + sv[1]) + (sv[2] + sv[3])) * (1.f / D_), 0.f) + LN_EPS, rs = __builtin_amdgcn_rsqf(v2);
        *sp = f32x4{v2, K0 * rs, K1 * rs * rs * rs, 0.f};
        asm volatile("s_waitcnt lgkmcnt(0)" ::: "memory");
        publish(5);
    } else asm volatile("s_barrier" ::: "memory");
    if (w < 4) __builtin_amdgcn_s_setprio(3); else if (!LAST) __builtin_amdgcn_s_setprio(2); else __builtin_amdgcn_s_setprio(1);
    STAMP(3, 1);
    u32x4 hb[4];
#pragma unroll
    for (int n = 0; n < 3; ++n) hb[n] = *(const LAS u32x4*)(hp + n * 1024);
#pragma unroll
    for (int k = 0; k < 18; ++k) {
        if (k == 0) asm volatile("s_waitcnt vmcnt(9)" : "+v"(wf[0][0]), "+v"(wf[0][1]), "+v"(wf[0][2]));
#pragma unroll
        for (int m = 0; m < 8; ++m) {
            const int n = k * 8 + m;
            if ((n + 3) % 24 == 12) {
                unsigned tv, ts;
                asm volatile("v_mov_b32 %0, %2\n.Lk4spin%=:\n\tds_read_b32 %0, %0\n\ts_waitcnt lgkmcnt(0)\n\tv_readfirstlane_b32 %1, %0\n\tv_mov_b32 %0, %2\n\ts_nop 2\n\t"
                             "s_bitcmp1_b32 %1, %3\n\ts_cbranch_scc1 .Lk4done%=\n\ts_sleep 1\n\ts_branch .Lk4spin%=\n.Lk4done%=:"
                             : "=&v"(tv), "=&s"(ts) : "s"(lds0 + L_FL), "i"(4 * ((n + 3) / 24) + 2) : "scc", "memory");
            }
            if (n && n % 18 == 0) *(volatile LAS unsigned*)(flag + 16 + w) = n / 9;
#define K4M(t) do { SBAR(); asm volatile("v_mfma_f32_16x16x32_bf16 %0, %1, %2, %0" : "+v"(acc[t][m]) : "v"(wf[k & 3][t]), "v"(hb[n & 3])); SBAR(); } while (0)
            K4M(0);
            if (n + 3 < 144) hb[(n + 3) & 3] = *(const LAS u32x4*)(hp + (n + 3) * 1024);
            K4M(1);
            if (n + 3 < 144) __builtin_amdgcn_s_waitcnt(0xC27F); else __builtin_amdgcn_s_waitcnt(0xC07F);
            K4M(2);
            if (k && k < 15 && m < 3) K4W("+v", k + 3, m);
            if (m == 7 && k < 17) asm volatile("s_waitcnt vmcnt(%3)" : "+v"(wf[(k + 1) & 3][0]), "+v"(wf[(k + 1) & 3][1]), "+v"(wf[(k + 1) & 3][2]) : "i"(k < 15 ? 6 : 3 * (16 - k)));
        }
        SBAR();
    }
    *(volatile LAS unsigned*)(flag + 16 + w) = CONS ? 16u : 255u;
    asm volatile("s_nop 7\n\ts_nop 1");
    SBAR();
    if (!LAST) __builtin_amdgcn_s_setprio(0);
    PIN(lo);
    const int l = lo >> 4, i16 = l & 15, q = l >> 4;
    STAMP(3, 2);
    f32x4 stv[8];
#pragma unroll
    for (int m = 0; m < 8; ++m) stv[m] = *(const LAS f32x4*)((const LAS char*)lds + L_ST + (m * 16 + i16) * 32 + (q & 1) * 16);
    SBAR();
    char* ad = ws + WS_AF + ((size_t)mtile * 144 + ((3 * W + q) >> 2)) * 8192 + ((3 * W + q) & 3) * 256 + i16 * 16 + 4096;
    const int kq = s >> 2, nb = s & 3;
    auto kso = [&](int p) { const int part = p / 12, j = p - 12 * part; return 9 * ((j / 3 + nb) & 3) + 3 * part + j % 3; };
    const char* asrc = ws + WS_AF + ((size_t)mtile * 144 + 36 * kq) * 8192 + (w - 4) * 2048;
    u32x4 af[6][2];
#define K5A(C, p) asm volatile("global_load_dwordx4 %0, %2, %3 sc1\n\tglobal_load_dwordx4 %1, %2, %3 offset:1024 sc1" : C(af[(p) % 6][0]), C(af[(p) % 6][1]) \
                               : "v"(lo), "s"(asrc + kso(p) * 8192) : "memory")
    unsigned fb = lds0 + L_FL, fv, rel;
    PIN(fb);
#define K5SPIN(st) asm volatile("s_cmp_eq_u64 %2, 0\n\ts_cbranch_scc1 .Lk5done%=\n.Lk5spin%=:\n\tds_read_b32 %0, %1 offset:%4\n\ts_waitcnt lgkmcnt(0)\n\t" \
                                "v_cmp_ge_u32 vcc, %3, %0\n\ts_cbranch_vccnz .Lk5spin%=\n.Lk5done%=:" \
                                : "=&v"(fv) : "v"(fb), "s"(late), "i"(st), "i"(128 + ((st) & 15) * 4) : "vcc", "scc", "memory")
#define K5GATE(g) do { unsigned gv_; asm volatile(".Lk5gate%=:\n\tds_read_b32 %0, %1 offset:20\n\ts_waitcnt lgkmcnt(0)\n\tv_cmp_ge_u32 vcc, %2, %0\n\ts_cbranch_vccnz .Lk5gate%=" \
                                                 : "=&v"(gv_) : "v"(fb), "i"(g) : "vcc", "memory"); } while (0)
    unsigned long long late = 1;
    auto finish = [&](int m, const unsigned (&d)[3]) __attribute__((always_inline)) {
        const auto s16 = __builtin_amdgcn_permlane16_swap(d[2], d[2], false, false);
        const auto sA = __builtin_amdgcn_permlane32_swap(d[0], s16[0], false, false);
        const auto sB = __builtin_amdgcn_permlane32_swap(d[1], s16[1], false, false);
        asm volatile("s_mov_b64 exec, %2\n\tglobal_store_dwordx4 %0, %1, off offset:%3 sc1\n\ts_mov_b64 exec, -1\n\ts_nop 0"
                     :: "v"(ad), "v"(u32x4{sA[0], sA[1], sB[0], sB[1]}), "s"(0xffffffffffffull), "i"(m * 1024 - 4096) : "memory");
    };
    GAS unsigned* ct = (GAS unsigned*)(ws + WS_CT) + (mtile * 4 + kq) * 256;
    {
#pragma unroll
        for (int m = 0; m < 8; ++m) {
            const f32x4 st = stv[m];
            unsigned d[3];
            float G[6], U[6], E[6];
#pragma unroll
            for (int i = 0; i < 6; ++i) { G[i] = acc[i >> 1][m][i & 1]; U[i] = acc[i >> 1][m][2 + (i & 1)]; }
            asm volatile("v_mul_f32 %0, %12, %12\n\t" "v_mul_f32 %1, %13, %13\n\t" "v_mul_f32 %2, %14, %14\n\t" "v_mul_f32 %3, %15, %15\n\t" "v_mul_f32 %4, %16, %16\n\t" "v_mul_f32 %5, %17, %17\n\t"
                         "v_fma_f32 %0, %0, %20, %19\n\t" "v_fma_f32 %1, %1, %20, %19\n\t" "v_fma_f32 %2, %2, %20, %19\n\t" "v_fma_f32 %3, %3, %20, %19\n\t" "v_fma_f32 %4, %4, %20, %19\n\t" "v_fma_f32 %5, %5, %20, %19\n\t"
                         "v_mul_f32 %0, %0, %12\n\t" "v_mul_f32 %1, %1, %13\n\t" "v_mul_f32 %2, %2, %14\n\t" "v_mul_f32 %3, %3, %15\n\t" "v_mul_f32 %4, %4, %16\n\t" "v_mul_f32 %5, %5, %17\n\t"
                         "v_exp_f32 %0, %0\n\tv_mul_f32 %6, %6, %12\n\t" "v_exp_f32 %1, %1\n\tv_mul_f32 %7, %7, %13\n\t" "v_exp_f32 %2, %2\n\tv_mul_f32 %8, %8, %14\n\t" "v_exp_f32 %3, %3\n\tv_mul_f32 %9, %9, %15\n\t" "v_exp_f32 %4, %4\n\tv_mul_f32 %10, %10, %16\n\t" "v_exp_f32 %5, %5\n\tv_mul_f32 %11, %11, %17\n\t"
                         "v_fma_f32 %0, %0, %18, %18\n\t" "v_fma_f32 %1, %1, %18, %18\n\t" "v_fma_f32 %2, %2, %18, %18\n\t" "v_fma_f32 %3, %3, %18, %18\n\t" "v_fma_f32 %4, %4, %18, %18\n\t" "v_fma_f32 %5, %5, %18, %18\n\t"
                         "v_rcp_f32 %0, %0\n\t" "v_rcp_f32 %1, %1\n\t" "v_rcp_f32 %2, %2\n\t" "v_rcp_f32 %3, %3\n\t" "v_rcp_f32 %4, %4\n\t" "v_rcp_f32 %5, %5\n\t"
                         "v_mul_f32 %6, %6, %0\n\t" "v_mul_f32 %7, %7, %1\n\t" "v_mul_f32 %8, %8, %2\n\t" "v_mul_f32 %9, %9, %3\n\t" "v_mul_f32 %10, %10, %4\n\t" "v_mul_f32 %11, %11, %5\n\t"
                         : "=&v"(E[0]), "=&v"(E[1]), "=&v"(E[2]), "=&v"(E[3]), "=&v"(E[4]), "=&v"(E[5]), "+v"(U[0]), "+v"(U[1]), "+v"(U[2]), "+v"(U[3]), "+v"(U[4]), "+v"(U[5])
                         : "v"(G[0]), "v"(G[1]), "v"(G[2]), "v"(G[3]), "v"(G[4]), "v"(G[5]), "v"(st[0]), "v"(st[1]), "v"(st[2]));
#pragma unroll
            for (int t = 0; t < 3; ++t) d[t] = pk2(U[2 * t], U[2 * t + 1]);
            finish(m, d);
            if (CONS && m >= 5) {
                if (m == 5) K5GATE(0);
                K5A("=&v", 2 * m - 10); K5A("=&v", 2 * m - 9);
            }
        }
    }
    STAMP(3, 4);
    if (!CONS) {
        asm volatile("s_waitcnt vmcnt(0)" ::: "memory");
        if (l == 0) {
            __hip_atomic_fetch_add(ct + (w >> 2) * 64, 1u, __ATOMIC_RELAXED, __HIP_MEMORY_SCOPE_AGENT);
            __hip_atomic_fetch_add(flag + 1 + (w >> 2), 64u, __ATOMIC_RELAXED, __HIP_MEMORY_SCOPE_WORKGROUP);
        }
        STAMP(3, 3);
        if (LAST) return;
        const volatile LAS unsigned* allow = flag + 16 + (l < 12 ? l : 0);
        const char* wsrc = ws + WS_WD + ((size_t)nb * 144 + 36 * kq) * 9216;
        if (w == 3) {
#pragma unroll 1
            for (int g = 0; g < 5; ++g) {
                if (g & 1) while (__any(*(const volatile LAS unsigned*)(flag + 1 + (g + 1) / 2) < 256u)) __builtin_amdgcn_s_sleep(2);
                else poll_rmw<4>(ct + (g / 2) * 64, 16u);
                *(volatile LAS unsigned*)(flag + 5) = g + 1;
            }
            return;
        }
        poll_ge<16>((GAS unsigned*)(ws + WS_CT) + (672 + (b & 63)) * 64, 1u);
#pragma unroll 1
        for (int p = w; p < 36; p += 3) {
            while (__any(*allow <= (unsigned)p)) __builtin_amdgcn_s_sleep(2);
            asm volatile("" ::: "memory");
#pragma unroll
            for (int t = 0; t < 9; t += 3) gldsn_dev<3>(wsrc + kso(p) * 9216 + t * 1024, lo, lds0 + (p & 15) * 9216 + t * 1024);
            asm volatile("s_waitcnt vmcnt(0)" ::: "memory");
            *(volatile LAS unsigned*)(flag + 32 + (p & 15)) = p + 1;
        }
        return;
    }
    __builtin_amdgcn_s_setprio(2);
    const int mp = w - 4;
    f32x4 ac[2][9];
    u32x4 bf[9];
    const LAS char* rb[3] = {(const LAS char*)lds + lo, (const LAS char*)lds + lo + 6 * 9216, (const LAS char*)lds + lo + 12 * 9216};
    PIN(rb[1]); PIN(rb[2]);
    const unsigned aw = fb + 64 + 4 * w;
    K5SPIN(0);
#pragma unroll
    for (int t = 0; t < 9; ++t) bf[t] = *(const LAS u32x4*)(rb[0] + t * 1024);
    K5SPIN(1);
    STAMP(4, 1);
#pragma unroll
    for (int p = 0; p < 36; ++p) {
        asm volatile("s_waitcnt vmcnt(%2)" : "+v"(af[p % 6][0]), "+v"(af[p % 6][1]) : "i"(p < 4 ? 12 - p / 2 : (p < 31 ? 10 : 2 * (35 - p))));
#pragma unroll
        for (int i = 0; i < 18; ++i) {
            const int t = i >> 1;
            if (i % 6 == 0) { if (p < 35) __builtin_amdgcn_s_waitcnt(0xC67F); else __builtin_amdgcn_s_waitcnt(0xC07F); }
            SBAR();
            if (p) asm volatile("v_mfma_f32_16x16x32_bf16 %0, %1, %2, %0" : "+v"(ac[i & 1][t]) : "v"(bf[t]), "v"(af[p % 6][i & 1]));
            else asm volatile("v_mfma_f32_16x16x32_bf16 %0, %1, %2, 0" : "=&v"(ac[i & 1][t]) : "v"(bf[t]), "v"(af[p % 6][i & 1]));
            SBAR();
            if ((i & 1) && p < 35) bf[t] = *(const LAS u32x4*)(rb[((p + 1) & 15) / 6] + (((p + 1) & 15) % 6) * 9216 + t * 1024);
            if (p < 34) {
                if (i == 0) asm volatile("ds_read_b32 %0, %1 offset:%2" : "=v"(fv) : "v"(fb), "i"(128 + ((p + 2) & 15) * 4) : "memory");
                if (i == 14) asm volatile("v_cmp_ge_u32 %0, %2, %1" : "=s"(late) : "v"(fv), "i"(p + 2));
                if (i == 16) K5SPIN(p + 2);
            }
            if (p < 20) {
                if (i == 10) asm volatile("v_mov_b32 %0, %1" : "=v"(rel) : "i"(p + 17));
                if (i == 12) asm volatile("ds_write_b32 %0, %1" :: "v"(aw), "v"(rel) : "memory");
            }
            if (p == 2 && i == 8) {
                unsigned z;
                asm volatile("v_mov_b32 %0, 1\n\tv_mov_b32 %1, 0\n\ts_waitcnt vmcnt(8)\n\tds_add_u32 %3, %0 offset:8\n\ts_mov_b64 exec, 1\n\tglobal_atomic_add %1, %0, %2\n\ts_mov_b64 exec, -1"
                             : "=&v"(rel), "=&v"(z) : "s"(ct + 64), "v"(fb) : "memory");
            }
        }
        if (p % 12 == 6 || p % 12 == 9) { if (p < 30) K5GATE(2 * (p / 12) + (p % 12) / 3 - 1); }
        if (p < 30) K5A("+v", p + 6);
        SBAR();
    }
    asm volatile("s_nop 7\n\ts_nop 1");
    SBAR();
    STAMP(4, 2);
    PIN(lo);
    const int i2 = (lo >> 4) & 15, q2 = lo >> 8;
    char* ex = ws + WS_EX + ((size_t)((mtile * 4 + nb) * 4 + mp) * 4) * 9216 + lo;
    const bool x8 = mp == kq;
#pragma unroll
    for (int u = 0; u < 9; ++u) {
        u32x4 v;
#pragma unroll
        for (int j = 0; j < 4; ++j) {
            const int a = u >> 2, g = (u >> 1) & 1, c = u & 1;
            v[j] = u < 8 ? pk2(ac[a][4 * g + 2 * (j & 1)][2 * c + (j >> 1)], ac[a][4 * g + 2 * (j & 1) + 1][2 * c + (j >> 1)])
                         : pk2(ac[j >> 1][8][2 * (j & 1)], ac[j >> 1][8][2 * (j & 1) + 1]);
        }
        if (u < 8 ? (u >> 1) != kq : !x8) st_wt(ex + kq * 9216 + u * 1024, v);
    }
    STAMP(4, 4);
    const int row = mp * 32 + (kq >> 1) * 16 + i2;
    const char* hr = ws + WS_HF + (size_t)mtile * 147456 + i2 * 16;
    auto hf = [&](int c, int mt) { const int kap = 18 * nb + c; return hr + (kap >> 2) * 8192 + (kap & 3) * 256 + mt * 1024; };
    u32x4 rs[3];
    {
        u32x2 r0, r1;
        asm volatile("global_load_dwordx4 %0, %4, off sc1\n\tglobal_load_dwordx4 %1, %5, off sc1\n\tglobal_load_dwordx2 %2, %6, off sc1\n\tglobal_load_dwordx2 %3, %6, off offset:1024 sc1\n\t"
                     "s_waitcnt vmcnt(0)" : "=&v"(rs[0]), "=&v"(rs[1]), "=&v"(r0), "=&v"(r1)
                     : "v"(hf(8 * (kq & 1) + 2 * q2, row >> 4)), "v"(hf(8 * (kq & 1) + 2 * q2 + 1, row >> 4)), "v"(hf(16 + (q2 >> 1), 2 * mp) + (q2 & 1) * 8) : "memory");
        rs[2] = u32x4{r0[0], r0[1], r1[0], r1[1]};
    }
    GAS unsigned* cx = (GAS unsigned*)(ws + WS_CT) + (256 + (mtile * 4 + nb) * 4 + mp) * 64;
    if (lo == 0) __hip_atomic_fetch_add(cx, 1u, __ATOMIC_RELAXED, __HIP_MEMORY_SCOPE_AGENT);
    float own[3][8];
#pragma unroll
    for (int i = 0; i < 3; ++i)
#pragma unroll
        for (int e = 0; e < 8; ++e) {
            const int t = e & 3, r = 2 * i + (e >> 2);
            const float v = i == 2 ? ac[e >> 2][8][e & 3] : (kq == 0 ? ac[0][t][r & 3] : kq == 1 ? ac[0][4 + t][r & 3] : kq == 2 ? ac[1][t][r & 3] : ac[1][4 + t][r & 3]);
            own[i][e] = v + ((e & 1) ? bfhi(rs[i][e >> 1]) : bflo(rs[i][e >> 1]));
        }
    poll_rmw(cx, 4u);
    STAMP(4, 5);
    u32x4 pv[3][3];
#pragma unroll
    for (int j = 0; j < 3; ++j)
#pragma unroll
        for (int i = 0; i < 3; ++i)
            asm volatile("global_load_dwordx4 %0, %1, off sc1" : "=v"(pv[j][i]) : "v"(ex + ((kq + 1 + j) & 3) * 9216 + (i < 2 ? 2 * kq + i : (x8 ? 8 : 2 * kq)) * 1024) : "memory");
    asm volatile("s_waitcnt vmcnt(0)" : "+v"(pv[0][0]), "+v"(pv[0][1]), "+v"(pv[0][2]), "+v"(pv[1][0]), "+v"(pv[1][1]), "+v"(pv[1][2]), "+v"(pv[2][0]), "+v"(pv[2][1]), "+v"(pv[2][2]));
    u32x4 o[3];
#pragma unroll
    for (int i = 0; i < 3; ++i)
#pragma unroll
        for (int j = 0; j < 4; ++j)
            o[i][j] = pk2((own[i][2 * j] + bflo(pv[0][i][j])) + (bflo(pv[1][i][j]) + bflo(pv[2][i][j])), (own[i][2 * j + 1] + bfhi(pv[0][i][j])) + (bfhi(pv[1][i][j]) + bfhi(pv[2][i][j])));
    char* op = (char*)out + ((size_t)(mtile * 128) * D_ + 144 * nb) * 2;
    *(GAS u32x4*)(op + row * (D_ * 2) + (kq & 1) * 128 + q2 * 32) = o[0];
    *(GAS u32x4*)(op + row * (D_ * 2) + (kq & 1) * 128 + q2 * 32 + 16) = o[1];
    if (x8) {
        *(GAS u32x2*)(op + (mp * 32 + i2) * (D_ * 2) + 256 + q2 * 8) = u32x2{o[2][0], o[2][1]};
        *(GAS u32x2*)(op + (mp * 32 + 16 + i2) * (D_ * 2) + 256 + q2 * 8) = u32x2{o[2][2], o[2][3]};
    }
    STAMP(4, 3);
}

extern "C" __global__ __launch_bounds__(768, 3) void k2_main(const u16* __restrict__ x, const u16* __restrict__ wo, const u16* __restrict__ wg, const u16* __restrict__ wu,
                                                             const u16* __restrict__ wd, char* __restrict__ ws, u16* __restrict__ out) {
    __shared__ __attribute__((aligned(16))) char lds[163840];
    const int w = __builtin_amdgcn_readfirstlane(threadIdx.x >> 6);
    STAMP(1, 0);
    STAMP(2, 0);
    if (w < 4) k2_attn(x, ws, lds, w); else k2_helper(wo, wg, wu, wd, ws, lds, w);
    if ((w >> 2) == 1) k4_body<true>(ws, out, lds, w); else k4_body<false>(ws, out, lds, w);
}

extern "C" __global__ void k9_end() {}

extern "C" void kernel_launch(void* const* d_in, const int* in_sizes, int n_in, void* d_out, int out_size, void* d_ws, size_t ws_size,
                              hipStream_t stream) {
    const u16* x = (const u16*)d_in[0];
    char* ws = (char*)d_ws;
    k1_qkv<<<256, 512, 0, stream>>>(x, (const u16*)d_in[1], ws);
    k2_main<<<256, 768, 0, stream>>>(x, (const u16*)d_in[2], (const u16*)d_in[4], (const u16*)d_in[5], (const u16*)d_in[6], ws, (u16*)d_out);
}
```
